```python
import torch

def _softmax(x, axis=-1):
    return torch.softmax(x, dim=axis)
def _gelu(x):
    return torch.nn.functional.gelu(x, approximate="tanh")
def _silu(x):
    return torch.nn.functional.silu(x)
def _sigmoid(x):
    return torch.sigmoid(x)
def _layernorm(x, axis=-1, eps=1e-5):
    m = x.mean(dim=axis, keepdim=True)
    v = x.var(dim=axis, keepdim=True, unbiased=False)
    return (x - m) / torch.sqrt(v + eps)
def _scatter_add(tgt, idx, src, axis=0):
    return tgt.clone().index_add_(axis, idx, src)
def _segment_sum(src, seg, n):
    out = torch.zeros((n,) + src.shape[1:], dtype=src.dtype)
    return out.index_add_(0, seg, src)
def _pad(x, pw):
    flat = tuple(v for pair in reversed(pw) for v in pair)
    return torch.nn.functional.pad(x, flat)

kernel_name = 'sequential_preLN__ssm_conv_s4d__mixer_token_mlp__prompt'

def setup_inputs():

    _w = (lambda a: (lambda t: t.to(torch.bfloat16) if t.is_floating_point() else t)(torch.as_tensor(a)))
    def _A_setup():
        Bsz, L, D, N = 1, 2048, 640, 128
        return {
            "u": _w((lambda *s: torch.randn(*s, dtype=torch.bfloat16) * 0.05)(Bsz, L, D)),
            "log_A_real": _w((lambda *s: torch.randn(*s, dtype=torch.bfloat16) * 6.4)(D, N) * 0.5),
            "A_imag": _w((lambda *s: torch.randn(*s, dtype=torch.bfloat16) * 0.05)(D, N) * 3.14),
            "C_real": _w((lambda *s: torch.randn(*s, dtype=torch.bfloat16) * 0.05)(D, N) / N ** 0.5),
            "C_imag": _w((lambda *s: torch.randn(*s, dtype=torch.bfloat16) * 0.05)(D, N) / N ** 0.5),
            "log_dt": _w((lambda *s: torch.randn(*s, dtype=torch.bfloat16) * 0.05)(D) * 0.5 - 4.0),
            "D_skip": _w((lambda *s: torch.randn(*s, dtype=torch.bfloat16) * 0.05)(D)),
        }
    _A_in = _A_setup()

    def _B_setup():
        B, S, D, F = 1, 2048, 640, 1728
        return {
            "x": _w((lambda *s: torch.randn(*s, dtype=torch.bfloat16) * 0.05)(B, S, D)),
            "w1": _w((lambda *s: torch.randn(*s, dtype=torch.bfloat16) * 0.4)(S, F) * (S ** -0.5)),
            "b1": _w((lambda *s: torch.randn(*s, dtype=torch.bfloat16) * 12.8)(F) * 0.02),
            "w2": _w((lambda *s: torch.randn(*s, dtype=torch.bfloat16) * 0.2)(F, S) * (F ** -0.5)),
            "b2": _w((lambda *s: torch.randn(*s, dtype=torch.bfloat16) * 3.2)(S) * 0.02),
        }
    _B_in = _B_setup()

    return {
        "A_u": _A_in["u"],
        "A_log_A_real": _A_in["log_A_real"],
        "A_A_imag": _A_in["A_imag"],
        "A_C_real": _A_in["C_real"],
        "A_C_imag": _A_in["C_imag"],
        "A_log_dt": _A_in["log_dt"],
        "A_D_skip": _A_in["D_skip"],
        "B_x": _B_in["x"],
        "B_w1": _B_in["w1"],
        "B_b1": _B_in["b1"],
        "B_w2": _B_in["w2"],
        "B_b2": _B_in["b2"],
    }

def reference(A_u, A_log_A_real, A_A_imag, A_C_real, A_C_imag, A_log_dt, A_D_skip, B_x, B_w1, B_b1, B_w2, B_b2):
    def _A_ref(u, log_A_real, A_imag, C_real, C_imag, log_dt, D_skip):
        L = u.shape[1]
        dt = torch.exp(log_dt)
        A = -torch.exp(log_A_real) + 1j * A_imag
        C = (C_real + 1j * C_imag) * dt[:, None]
        pos = (lambda x, dt: x.to(dt))(torch.arange(L), log_dt.dtype)
        P = torch.exp((A * dt[:, None])[:, :, None] * pos[None, None, :])
        K = 2.0 * torch.einsum("dn,dnl->dl", C, P).real
        L2 = 2 * L
        u_t = (lambda x, *d: x.permute(*d))(u, 0, 2, 1)
        Kf = (lambda x, *a, **k: torch.fft.rfft(x.float(), *a, **k))(K, L2)
        Uf = (lambda x, *a, **k: torch.fft.rfft(x.float(), *a, **k))(u_t, L2)
        y = (lambda x, *a, **k: torch.fft.irfft(x, *a, **k).to(torch.bfloat16))(Uf * Kf[None, :, :], L2)[:, :, :L]
        y = (lambda x, *d: x.permute(*d))(y, 0, 2, 1)
        return y + u * D_skip[None, None, :]

    def _B_ref(x, w1, b1, w2, b2):
        B, S, D = x.shape
        h = _layernorm(x, axis=-1)
        u = torch.einsum("bsd,sf->bdf", h, w1) + b1
        u = _gelu(u)
        y = torch.einsum("bdf,fs->bds", u, w2) + b2
        return (lambda x, *d: x.permute(*d))(y, 0, 2, 1)

    x = A_u
    h = x + _A_ref(_layernorm(x, axis=-1), A_log_A_real, A_A_imag, A_C_real, A_C_imag, A_log_dt, A_D_skip)
    h2 = h + _B_ref(_layernorm(h, axis=-1), B_w1, B_b1, B_w2, B_b2)
    return h2
```

```cpp
#include <hip/hip_runtime.h>
#include <hip/hip_bf16.h>
#include <stdint.h>

typedef unsigned short u16;
typedef unsigned int u32;
typedef u32 u32x2 __attribute__((ext_vector_type(2)));
typedef u32 u32x4 __attribute__((ext_vector_type(4)));
typedef float f32x2 __attribute__((ext_vector_type(2)));
typedef float f32x4 __attribute__((ext_vector_type(4)));
typedef float f32x16 __attribute__((ext_vector_type(16)));
typedef __bf16 bf16x2 __attribute__((ext_vector_type(2)));
typedef __bf16 bf16x4 __attribute__((ext_vector_type(4)));
typedef __bf16 bf16x8 __attribute__((ext_vector_type(8)));
typedef _Float16 f16x2 __attribute__((ext_vector_type(2)));
typedef _Float16 f16x8 __attribute__((ext_vector_type(8)));
#define LAS __attribute__((address_space(3)))

constexpr int L = 2048, D = 640, NM = 128, F = 1728;
constexpr float EPS = 1e-5f;

constexpr size_t SZ_ACT = (size_t)L * D * 2;
constexpr size_t OFF_U = 0;
constexpr size_t OFF_K = OFF_U + SZ_ACT;
constexpr size_t OFF_Y = OFF_K + SZ_ACT + 4096;
constexpr size_t OFF_HN = OFF_Y + SZ_ACT;
constexpr size_t OFF_H = OFF_HN + SZ_ACT;
constexpr size_t OFF_G = OFF_H + SZ_ACT;

__device__ __forceinline__ float bflo(u32 v) { return __builtin_bit_cast(float, v << 16); }
__device__ __forceinline__ float bfhi(u32 v) { return __builtin_bit_cast(float, v & 0xffff0000u); }
__device__ __forceinline__ float bf2f(u16 v) { return __builtin_bit_cast(float, (u32)v << 16); }
__device__ __forceinline__ u32 pk_bf16(float lo, float hi) {
  f32x2 a = {lo, hi};
  return __builtin_bit_cast(u32, __builtin_convertvector(a, bf16x2));
}
__device__ __forceinline__ float rbf(float x) { return bflo(pk_bf16(x, x)); }
__device__ __forceinline__ u32 pk_f16(float lo, float hi) {
  f32x2 a = {lo, hi};
  return __builtin_bit_cast(u32, __builtin_convertvector(a, f16x2));
}
template <int CTRL> __device__ __forceinline__ float dpp(float x) {
  return __builtin_bit_cast(float, __builtin_amdgcn_mov_dpp(__builtin_bit_cast(int, x), CTRL, 0xf, 0xf, true));
}
template <int CTRL> __device__ __forceinline__ u32 dppu(u32 x) {
  return (u32)__builtin_amdgcn_mov_dpp((int)x, CTRL, 0xf, 0xf, true);
}
constexpr int XOR1 = 0xB1, XOR2 = 0x4E, ROR8 = 0x128, ROR4 = 0x124;
__device__ __forceinline__ float xrow_sum(float x) {
  auto s = __builtin_amdgcn_permlane16_swap(__builtin_bit_cast(u32, x), __builtin_bit_cast(u32, x), false, false);
  x = __builtin_bit_cast(float, (u32)s[0]) + __builtin_bit_cast(float, (u32)s[1]);
  auto t = __builtin_amdgcn_permlane32_swap(__builtin_bit_cast(u32, x), __builtin_bit_cast(u32, x), false, false);
  return __builtin_bit_cast(float, (u32)t[0]) + __builtin_bit_cast(float, (u32)t[1]);
}
__device__ __forceinline__ float xrow_max(float x) {
  auto s = __builtin_amdgcn_permlane16_swap(__builtin_bit_cast(u32, x), __builtin_bit_cast(u32, x), false, false);
  x = fmaxf(__builtin_bit_cast(float, (u32)s[0]), __builtin_bit_cast(float, (u32)s[1]));
  auto t = __builtin_amdgcn_permlane32_swap(__builtin_bit_cast(u32, x), __builtin_bit_cast(u32, x), false, false);
  return fmaxf(__builtin_bit_cast(float, (u32)t[0]), __builtin_bit_cast(float, (u32)t[1]));
}
__device__ __forceinline__ float tok_sum(float x) {
  x += dpp<ROR4>(x);
  x += dpp<ROR8>(x);
  return xrow_sum(x);
}
__device__ __forceinline__ f32x4 mfma16(u32x4 a, u32x4 b, f32x4 c) {
  return __builtin_amdgcn_mfma_f32_16x16x32_bf16(__builtin_bit_cast(bf16x8, a), __builtin_bit_cast(bf16x8, b), c, 0, 0, 0);
}
__device__ __forceinline__ u32x2 tr_read(const char* lds_ptr) {
  return __builtin_bit_cast(u32x2, __builtin_amdgcn_ds_read_tr16_b64_v4bf16((LAS bf16x4*)lds_ptr));
}

__device__ __forceinline__ void layernorm40(float (&v)[40]) {
  float s = 0.f;
#pragma unroll
  for (int i = 0; i < 40; ++i) s += v[i];
  const float mean = tok_sum(s) * (1.0f / D);
  float q = 0.f;
#pragma unroll
  for (int i = 0; i < 40; ++i) { const float t = v[i] - mean; q += t * t; }
  const float var = tok_sum(q) * (1.0f / D);
  const float mb = rbf(mean);
  const float den = rbf(__builtin_sqrtf(rbf(rbf(var) + EPS)));
  const float rd = 1.0f / den;
#pragma unroll
  for (int i = 0; i < 40; i += 2) {
    const u32 c = pk_bf16(v[i] - mb, v[i + 1] - mb);
    const u32 o = pk_bf16(bflo(c) * rd, bfhi(c) * rd);
    v[i] = bflo(o); v[i + 1] = bfhi(o);
  }
}

constexpr int TROW = 1344;

__device__ __forceinline__ void store_t8(const float (&v)[40], char* smem, u32x4* dst, int wave, int lane) {
  char* wp = smem + (4 * wave + (lane & 3)) * TROW + (lane >> 2) * 8;
#pragma unroll
  for (int i = 0; i < 10; ++i) {
    u32x2 o;
    o.x = __builtin_amdgcn_perm(__builtin_bit_cast(u32, v[4 * i + 1]), __builtin_bit_cast(u32, v[4 * i]), 0x07060302);
    o.y = __builtin_amdgcn_perm(__builtin_bit_cast(u32, v[4 * i + 3]), __builtin_bit_cast(u32, v[4 * i + 2]), 0x07060302);
    *(u32x2*)(wp + i * 128) = o;
  }
  __syncthreads();
  const char* rp = smem + ((lane & 15) >> 2) * TROW + (lane >> 4) * 32 + (lane & 3) * 8 + wave * 128;
#pragma unroll
  for (int i = 0; i < 5; ++i) {
    const u32x2 a = tr_read(rp + i * 256);
    const u32x2 b = tr_read(rp + i * 256 + 4 * TROW);
    u32x4 o = {a.x, a.y, b.x, b.y};
    dst[i * 128 + wave * 64 + lane] = o;
  }
}

struct cplx { float re, im; };
__device__ __forceinline__ cplx cmul(cplx a, cplx b) {
  cplx r;
  r.re = a.re * b.re - a.im * b.im;
  r.im = a.re * b.im + a.im * b.re;
  return r;
}
__device__ __forceinline__ cplx cexp_p(float a2, float brev, float p) {
  const float mag = __builtin_amdgcn_exp2f(a2 * p);
  const float ang = __builtin_amdgcn_fractf(brev * p);
  cplx r;
  r.re = mag * __builtin_amdgcn_cosf(ang);
  r.im = mag * __builtin_amdgcn_sinf(ang);
  return r;
}

constexpr int KG_ROW = 272;
constexpr int KG_WAVE = (32 + 21) * KG_ROW;
constexpr int K1_LDS = 2 * KG_WAVE + 2 * 704 * 4;

__global__ __launch_bounds__(128) void k1_ln_kgen(const u16* __restrict__ x, const u16* __restrict__ logA, const u16* __restrict__ Aim,
                                                  const u16* __restrict__ Cre, const u16* __restrict__ Cim, const u16* __restrict__ logdt,
                                                  char* __restrict__ ws) {
  __shared__ __attribute__((aligned(16))) char smem[K1_LDS];
  const int lane = threadIdx.x & 63, wave = __builtin_amdgcn_readfirstlane(threadIdx.x >> 6);
  if (blockIdx.x < 256) {
    const int t = blockIdx.x * 8 + wave * 4 + (lane & 3);
    const u32x2* xp = (const u32x2*)(x + (size_t)t * D) + (lane >> 2);
    u32x2 raw[10];
#pragma unroll
    for (int i = 0; i < 10; ++i) raw[i] = xp[16 * i];
    float v[40];
#pragma unroll
    for (int i = 0; i < 10; ++i) { v[4 * i] = bflo(raw[i].x); v[4 * i + 1] = bfhi(raw[i].x); v[4 * i + 2] = bflo(raw[i].y); v[4 * i + 3] = bfhi(raw[i].y); }
    layernorm40(v);
    store_t8(v, smem, (u32x4*)(ws + OFF_U) + (size_t)blockIdx.x * D, wave, lane);
    return;
  }
  const int d = blockIdx.x - 256;
  const int idx = d * NM + wave * 64 + lane;
  const float dt = rbf(expf(bf2f(logdt[d])));
  const float are = -rbf(expf(bf2f(logA[idx])));
  const float a2 = (are * dt) * 1.44269504088896340736f;
  const float brev = (bf2f(Aim[idx]) * dt) * 0.15915494309189533577f;
  cplx c = {bf2f(Cre[idx]) * dt, bf2f(Cim[idx]) * dt};
  float mx = fmaxf(fabsf(c.re), fabsf(c.im));
  mx = fmaxf(mx, dpp<XOR1>(mx)); mx = fmaxf(mx, dpp<XOR2>(mx)); mx = fmaxf(mx, dpp<ROR4>(mx)); mx = fmaxf(mx, dpp<ROR8>(mx));
  mx = xrow_max(mx);
  const u32 eb = __builtin_bit_cast(u32, fmaxf(mx, 1e-30f)) & 0x7f800000u;
  const float unscale = __builtin_bit_cast(float, eb);
  const float scale = __builtin_bit_cast(float, 0x7f000000u - eb);
  c.re *= scale; c.im *= scale;
  const cplx z = cexp_p(a2, brev, 1.f), w = cexp_p(a2, brev, 8.f), w4 = cexp_p(a2, brev, 32.f), vv = cexp_p(a2, brev, 256.f);
  char* lo = smem + wave * KG_WAVE + lane * 4;
  char* hi = lo + 32 * KG_ROW;
  {
    cplx p[4];
    p[0] = {1.f, 0.f}; p[1] = w; p[2] = cmul(w, w); p[3] = cmul(p[2], w);
#pragma unroll
    for (int g = 0; g < 8; ++g) {
#pragma unroll
      for (int j = 0; j < 4; ++j) {
        *(u32*)(lo + (4 * g + j) * KG_ROW) = pk_f16(p[j].re, -p[j].im);
        if (g < 7) p[j] = cmul(p[j], w4);
      }
    }
  }
  {
    cplx ch[8];
    const cplx z2 = cmul(z, z);
    ch[0] = c; ch[1] = cmul(c, z);
#pragma unroll
    for (int h = 2; h < 8; ++h) ch[h] = cmul(ch[h - 2], z2);
#pragma unroll
    for (int h = 0; h < 8; ++h) *(u32*)(hi + h * KG_ROW) = pk_f16(ch[h].re, ch[h].im);
    cplx e0 = cmul(ch[0], vv), e2 = cmul(ch[2], vv), e4 = cmul(ch[4], vv), e6 = cmul(ch[6], vv);
    *(u32*)(hi + 8 * KG_ROW) = pk_f16(e0.re, e0.im);
    *(u32*)(hi + 9 * KG_ROW) = pk_f16(e2.re, e2.im);
    *(u32*)(hi + 10 * KG_ROW) = pk_f16(e4.re, e4.im);
    *(u32*)(hi + 11 * KG_ROW) = pk_f16(e6.re, e6.im);
    e0 = cmul(e0, vv); e4 = cmul(e4, vv);
    *(u32*)(hi + 12 * KG_ROW) = pk_f16(e0.re, e0.im);
    *(u32*)(hi + 13 * KG_ROW) = pk_f16(e4.re, e4.im);
    e0 = cmul(e0, vv); e4 = cmul(e4, vv);
    *(u32*)(hi + 14 * KG_ROW) = pk_f16(e0.re, e0.im);
    *(u32*)(hi + 15 * KG_ROW) = pk_f16(e4.re, e4.im);
#pragma unroll
    for (int r = 16; r < 21; ++r) { e0 = cmul(e0, vv); *(u32*)(hi + r * KG_ROW) = pk_f16(e0.re, e0.im); }
  }
  __builtin_amdgcn_fence(__ATOMIC_RELEASE, "wavefront");
  __builtin_amdgcn_wave_barrier();
  __builtin_amdgcn_fence(__ATOMIC_ACQUIRE, "wavefront");
  f32x16 acc = {};
  {
    const int r = lane & 31, hh = lane >> 5;
    const char* lp = smem + wave * KG_WAVE + r * KG_ROW + hh * 16;
    const char* hp = smem + wave * KG_WAVE + (32 + (r < 20 ? r : 20)) * KG_ROW + hh * 16;
#pragma unroll
    for (int ks = 0; ks < 8; ++ks) {
      const f16x8 a = *(const f16x8*)(hp + ks * 32);
      const f16x8 b = *(const f16x8*)(lp + ks * 32);
      acc = __builtin_amdgcn_mfma_f32_32x32x16_f16(a, b, acc, 0, 0, 0);
    }
  }
  float* tab = (float*)(smem + 2 * KG_WAVE) + wave * 704;
  {
    const int j = lane & 31, hh = lane >> 5;
    const float us = 2.0f * unscale;
    f32x4 o = {acc[0] * us, acc[1] * us, acc[2] * us, acc[3] * us};
    *(f32x4*)(tab + 8 * j + 4 * hh) = o;
    if (hh == 0) {
      f32x4 o1 = {acc[4] * us, acc[5] * us, acc[6] * us, acc[7] * us};
      *(f32x4*)(tab + 256 + 4 * j) = o1;
#pragma unroll
      for (int r = 0; r < 4; ++r) tab[512 + 32 * r + j] = acc[8 + r] * us;
    } else {
      f32x2 o2 = {acc[4] * us, acc[5] * us}, o3 = {acc[6] * us, acc[7] * us};
      *(f32x2*)(tab + 384 + 2 * j) = o2;
      *(f32x2*)(tab + 448 + 2 * j) = o3;
      if (j == 0) tab[640] = acc[8] * us;
    }
  }
  __syncthreads();
  {
    const float* t0 = (const float*)(smem + 2 * KG_WAVE);
    const int th = threadIdx.x;
    const int lhi = 2047 - 16 * th;
    const int s = lhi < 256 ? 0 : (lhi < 512 ? 1 : (lhi < 1024 ? 2 : 3));
    const int half = (1 << s) >> 1;
    float kv[16];
#pragma unroll
    for (int i = 0; i < 16; ++i) {
      const int l = lhi - i;
      const int q = s == 0 ? l : ((l + half - 1 + ((l >> s) & 1)) >> s);
      const int id = 128 * s + q;
      kv[i] = t0[id] + t0[704 + id];
    }
    u32x4 o0 = {pk_bf16(kv[0], kv[1]), pk_bf16(kv[2], kv[3]), pk_bf16(kv[4], kv[5]), pk_bf16(kv[6], kv[7])};
    u32x4 o1 = {pk_bf16(kv[8], kv[9]), pk_bf16(kv[10], kv[11]), pk_bf16(kv[12], kv[13]), pk_bf16(kv[14], kv[15])};
    u32x4* kp = (u32x4*)(ws + OFF_K + (size_t)d * 4096) + 2 * th;
    kp[0] = o0; kp[1] = o1;
  }
}

constexpr int CV_R = 4224;
constexpr int CV_U = 4640;
constexpr int CV_WAVE = 2 * CV_R + 2 * CV_U;
static_assert(CV_R % 256 == 128 && CV_U % 256 == 32, "bank layout");

__global__ __launch_bounds__(256) void k2_conv(char* __restrict__ ws) {
  extern __shared__ __attribute__((aligned(16))) char smem[];
  const int tid = threadIdx.x, lane = tid & 63, wave = __builtin_amdgcn_readfirstlane(tid >> 6);
  const int cg = (blockIdx.x & 7) * 20 + (blockIdx.x >> 3);
  const int d0 = cg * 4;
  u32x4 ur[4];
  {
    const u32x4* up = (const u32x4*)(ws + OFF_U) + (size_t)(tid >> 2) * D + d0 + (tid & 3);
#pragma unroll
    for (int i = 0; i < 4; ++i) ur[i] = up[(size_t)i * 64 * D];
  }
  u32x4 kr[4]; u32 kn[4];
  {
    const u32x4* kp = (const u32x4*)(ws + OFF_K + (size_t)(d0 + wave) * 4096) + lane;
#pragma unroll
    for (int i = 0; i < 4; ++i) { kr[i] = kp[64 * i]; kn[i] = *(const u32*)(kp + 64 * i + 1); }
    if (lane == 63) kn[3] = 0;
  }
  char* base = smem + wave * CV_WAVE;
  char* R0 = base; char* R1 = base + CV_R; char* U0 = base + 2 * CV_R; char* U1 = U0 + CV_U;
  {
    const u32x4 zz = {0, 0, 0, 0};
    if (lane < 4) { *(u32x4*)(R0 + 4096 + lane * 16) = zz; *(u32x4*)(R1 + 4096 + lane * 16) = zz; }
    if (lane < 28) { *(u32x4*)(U0 + lane * 16) = zz; *(u32x4*)(U1 + lane * 16) = zz; }
    if (lane == 28) *(u32*)(U0 + 448) = 0;
  }
  {
    char* ub = smem + (tid & 3) * CV_WAVE + 2 * CV_R + 448 + (tid >> 2) * 16;
#pragma unroll
    for (int i = 0; i < 4; ++i) {
      *(u32x4*)(ub + CV_U + i * 1024) = ur[i];
      *(u32*)(ub + i * 1024 + 4) = ur[i].x;
      u32x2 m = {ur[i].y, ur[i].z};
      *(u32x2*)(ub + i * 1024 + 8) = m;
      *(u32*)(ub + i * 1024 + 16) = ur[i].w;
    }
  }
#pragma unroll
  for (int i = 0; i < 4; ++i) {
    *(u32x4*)(R1 + (lane + 64 * i) * 16) = kr[i];
    u32x4 s = {__builtin_amdgcn_alignbit(kr[i].y, kr[i].x, 16), __builtin_amdgcn_alignbit(kr[i].z, kr[i].y, 16),
               __builtin_amdgcn_alignbit(kr[i].w, kr[i].z, 16), __builtin_amdgcn_alignbit(kn[i], kr[i].w, 16)};
    *(u32x4*)(R0 + (lane + 64 * i) * 16) = s;
  }
  __syncthreads();
  const int m = lane & 15, kg = lane >> 4;
  const char* tp = base + (m & 1) * CV_R + 2 * (2044 - 32 * 63 - 4 * (m >> 1) + 8 * kg);
  const char* up = U0 + (m & 1) * CV_U + 2 * (32 * (m >> 1) + 8 * kg);
  f32x4 acc[8];
#pragma unroll
  for (int a = 0; a < 8; ++a) acc[a] = (f32x4){0.f, 0.f, 0.f, 0.f};
#pragma unroll
  for (int f = 0; f < 8; ++f) {
    u32x4 T[8], U[8];
#pragma unroll
    for (int E = 0; E < 8; ++E) {
      const char* p = tp + 64 * (63 - (8 * E + f));
      const u32x2 a = *(const u32x2*)p, b = *(const u32x2*)(p + 8);
      T[E] = (u32x4){a.x, a.y, b.x, b.y};
      U[E] = *(const u32x4*)(up + 64 * (7 + 8 * E - f));
    }
#pragma unroll
    for (int E = 0; E < 8; ++E)
#pragma unroll
      for (int A = E; A < 8; ++A) acc[A] = mfma16(T[E], U[A - E], acc[A]);
  }
  __syncthreads();
  {
    char* yb = smem + (4 * (m >> 1) + kg) * 64 + wave * 16 + (m & 1) * 4;
#pragma unroll
    for (int A = 0; A < 8; ++A) {
      *(u32*)(yb + A * 2048) = pk_bf16(acc[A][0], acc[A][1]);
      *(u32*)(yb + A * 2048 + 8) = pk_bf16(acc[A][2], acc[A][3]);
    }
  }
  __syncthreads();
  {
    u32x4* yp = (u32x4*)(ws + OFF_Y) + (size_t)(tid >> 2) * D + d0 + (tid & 3);
#pragma unroll
    for (int i = 0; i < 4; ++i) yp[(size_t)i * 64 * D] = *(const u32x4*)(smem + tid * 16 + i * 4096);
  }
}

__global__ __launch_bounds__(128) void k3_ln2(const u16* __restrict__ x, const u16* __restrict__ dskip, char* __restrict__ ws) {
  __shared__ __attribute__((aligned(16))) char smem[8 * TROW + 10240];
  const int tid = threadIdx.x, lane = tid & 63, wave = __builtin_amdgcn_readfirstlane(tid >> 6);
  const int t = blockIdx.x * 8 + wave * 4 + (lane & 3);
  u32x4 yr[5];
  {
    const u32x4* yp = (const u32x4*)(ws + OFF_Y) + (size_t)blockIdx.x * D + tid;
#pragma unroll
    for (int i = 0; i < 5; ++i) yr[i] = yp[128 * i];
  }
  u32x2 raw[10], dr[10];
  {
    const u32x2* xp = (const u32x2*)(x + (size_t)t * D) + (lane >> 2);
    const u32x2* dp = (const u32x2*)dskip + (lane >> 2);
#pragma unroll
    for (int i = 0; i < 10; ++i) raw[i] = xp[16 * i];
#pragma unroll
    for (int i = 0; i < 10; ++i) dr[i] = dp[16 * i];
  }
  char* ys = smem + 8 * TROW;
#pragma unroll
  for (int i = 0; i < 5; ++i) *(u32x4*)(ys + (tid + 128 * i) * 16) = yr[i];
  float v[40];
#pragma unroll
  for (int i = 0; i < 10; ++i) { v[4 * i] = bflo(raw[i].x); v[4 * i + 1] = bfhi(raw[i].x); v[4 * i + 2] = bflo(raw[i].y); v[4 * i + 3] = bfhi(raw[i].y); }
  layernorm40(v);
  __syncthreads();
  {
    const char* rp = ys + (16 * (lane >> 4) + 4 * (lane & 3) + ((lane & 15) >> 2)) * 16 + wave * 8;
    u32x2* hp = (u32x2*)(ws + OFF_H + (size_t)t * D * 2) + (lane >> 2);
#pragma unroll
    for (int i = 0; i < 10; ++i) {
      const u32x2 yy = tr_read(rp + i * 1024);
      const u32 s0 = pk_bf16(v[4 * i] * bflo(dr[i].x), v[4 * i + 1] * bfhi(dr[i].x));
      const u32 s1 = pk_bf16(v[4 * i + 2] * bflo(dr[i].y), v[4 * i + 3] * bfhi(dr[i].y));
      const u32 a0 = pk_bf16(bflo(yy.x) + bflo(s0), bfhi(yy.x) + bfhi(s0));
      const u32 a1 = pk_bf16(bflo(yy.y) + bflo(s1), bfhi(yy.y) + bfhi(s1));
      u32x2 h;
      h.x = pk_bf16(bflo(raw[i].x) + bflo(a0), bfhi(raw[i].x) + bfhi(a0));
      h.y = pk_bf16(bflo(raw[i].y) + bflo(a1), bfhi(raw[i].y) + bfhi(a1));
      hp[16 * i] = h;
      v[4 * i] = bflo(h.x); v[4 * i + 1] = bfhi(h.x); v[4 * i + 2] = bflo(h.y); v[4 * i + 3] = bfhi(h.y);
    }
  }
  layernorm40(v);
  layernorm40(v);
  store_t8(v, smem, (u32x4*)(ws + OFF_HN) + (size_t)blockIdx.x * D, wave, lane);
}

constexpr int PD = 4;

template <int NSTEP, int LDW, bool W_IS_A>
__device__ __forceinline__ void gemm_body(__amdgpu_buffer_rsrc_t rsW, __amdgpu_buffer_rsrc_t rsA, int voffW, int voffA, int ks0,
                                          f32x4 (&acc)[5][4]) {
  u32x2 wr[PD][8];
  u32x4 ar[PD][5];
  auto issue = [&](int s, int slot) {
    const int sw = (ks0 + s) * 32 * LDW * 2, sa = (ks0 + s) * 4 * D * 16;
#pragma unroll
    for (int r = 0; r < 8; ++r) wr[slot][r] = __builtin_amdgcn_raw_buffer_load_b64(rsW, voffW, sw + r * LDW * 2, 0);
#pragma unroll
    for (int mt = 0; mt < 5; ++mt) ar[slot][mt] = __builtin_amdgcn_raw_buffer_load_b128(rsA, voffA + mt * 256, sa, 0);
  };
#pragma unroll
  for (int s = 0; s < PD; ++s) issue(s, s);
  __builtin_amdgcn_sched_barrier(0);
#pragma unroll
  for (int s = 0; s < NSTEP; ++s) {
    const int slot = s % PD;
    u32x4 wf[4];
#pragma unroll
    for (int jj = 0; jj < 4; ++jj) {
      wf[0][jj] = __builtin_amdgcn_perm(wr[slot][2 * jj + 1].x, wr[slot][2 * jj].x, 0x05040100);
      wf[1][jj] = __builtin_amdgcn_perm(wr[slot][2 * jj + 1].x, wr[slot][2 * jj].x, 0x07060302);
      wf[2][jj] = __builtin_amdgcn_perm(wr[slot][2 * jj + 1].y, wr[slot][2 * jj].y, 0x05040100);
      wf[3][jj] = __builtin_amdgcn_perm(wr[slot][2 * jj + 1].y, wr[slot][2 * jj].y, 0x07060302);
    }
#pragma unroll
    for (int mt = 0; mt < 5; ++mt)
#pragma unroll
      for (int c = 0; c < 4; ++c)
        acc[mt][c] = W_IS_A ? mfma16(wf[c], ar[slot][mt], acc[mt][c]) : mfma16(ar[slot][mt], wf[c], acc[mt][c]);
    __builtin_amdgcn_sched_barrier(0);
    if (s + PD < NSTEP) issue(s + PD, slot);
    __builtin_amdgcn_sched_barrier(0);
  }
}

__device__ __forceinline__ float gelu_tanh(float x) {
  const float z2 = x * (1.5957691216057308f + 0.07135481282624504f * x * x);
  const float e = __builtin_amdgcn_exp2f(-1.44269504088896340736f * z2);
  return x * __builtin_amdgcn_rcpf(1.0f + e);
}

__global__ __launch_bounds__(256, 1) void k4_gemm1(const u16* __restrict__ w1, const u16* __restrict__ b1, char* __restrict__ ws) {
  extern __shared__ __attribute__((aligned(16))) char smem[];
  const int tid = threadIdx.x, lane = tid & 63, wave = __builtin_amdgcn_readfirstlane(tid >> 6);
  const int xcd = blockIdx.x & 7, slot = blockIdx.x >> 3;
  const int mt0 = (xcd & 1) * 4 + (slot & 3), nt = (xcd >> 1) * 7 + (slot >> 2);
  if ((slot >> 2) >= 7 || nt >= 27) return;
  const int i = lane & 15, kg = lane >> 4;
  const __amdgpu_buffer_rsrc_t rsW = __builtin_amdgcn_make_buffer_rsrc((void*)w1, 0, L * F * 2, 0x00020000);
  const __amdgpu_buffer_rsrc_t rsA = __builtin_amdgcn_make_buffer_rsrc((void*)(ws + OFF_HN), 0, L * D * 2, 0x00020000);
  const int voffW = (8 * kg * F + nt * 64 + 4 * i) * 2;
  const int voffA = (kg * D + mt0 * 80 + i) * 16;
  f32x4 acc[5][4];
#pragma unroll
  for (int a = 0; a < 5; ++a)
#pragma unroll
    for (int c = 0; c < 4; ++c) acc[a][c] = (f32x4){0.f, 0.f, 0.f, 0.f};
  gemm_body<16, F, true>(rsW, rsA, voffW, voffA, wave * 16, acc);
  f32x4 own[5];
#pragma unroll
  for (int r = 0; r < 4; ++r) {
#pragma unroll
    for (int mt = 0; mt < 5; ++mt) {
      const f32x4 v = {acc[mt][0][r], acc[mt][1][r], acc[mt][2][r], acc[mt][3][r]};
      if (r == wave) own[mt] = v;
      else *(f32x4*)(smem + ((r * 4 + wave) * 5 + mt) * 1024 + lane * 16) = v;
    }
  }
  const u32x2 bb = *(const u32x2*)(b1 + nt * 64 + 16 * kg + 4 * wave);
  __syncthreads();
#pragma unroll
  for (int o = 1; o < 4; ++o) {
    const int src = (wave + o) & 3;
#pragma unroll
    for (int mt = 0; mt < 5; ++mt) own[mt] += *(const f32x4*)(smem + ((wave * 4 + src) * 5 + mt) * 1024 + lane * 16);
  }
  char* gp = ws + OFF_G + ((size_t)(nt * 8 + 2 * kg + (wave >> 1)) * D + mt0 * 80 + i) * 16 + (wave & 1) * 8;
#pragma unroll
  for (int mt = 0; mt < 5; ++mt) {
    const u32 p0 = pk_bf16(own[mt][0], own[mt][1]), p1 = pk_bf16(own[mt][2], own[mt][3]);
    const u32 q0 = pk_bf16(bflo(p0) + bflo(bb.x), bfhi(p0) + bfhi(bb.x)), q1 = pk_bf16(bflo(p1) + bflo(bb.y), bfhi(p1) + bfhi(bb.y));
    u32x2 o;
    o.x = pk_bf16(gelu_tanh(bflo(q0)), gelu_tanh(bfhi(q0)));
    o.y = pk_bf16(gelu_tanh(bflo(q1)), gelu_tanh(bfhi(q1)));
    *(u32x2*)(gp + mt * 256) = o;
  }
}

__global__ __launch_bounds__(256, 1) void k5_gemm2(const u16* __restrict__ w2, const u16* __restrict__ b2, char* __restrict__ ws,
                                                   u16* __restrict__ out) {
  extern __shared__ __attribute__((aligned(16))) char smem[];
  const int tid = threadIdx.x, lane = tid & 63, wave = __builtin_amdgcn_readfirstlane(tid >> 6);
  const int xcd = blockIdx.x & 7, slot = blockIdx.x >> 3;
  const int mt0 = (xcd & 1) * 4 + (slot & 3), nt = (xcd >> 1) * 8 + (slot >> 2);
  const int i = lane & 15, kg = lane >> 4;
  const __amdgpu_buffer_rsrc_t rsW = __builtin_amdgcn_make_buffer_rsrc((void*)w2, 0, L * F * 2, 0x00020000);
  const __amdgpu_buffer_rsrc_t rsA = __builtin_amdgcn_make_buffer_rsrc((void*)(ws + OFF_G), 0, F * D * 2, 0x00020000);
  const int voffW = (8 * kg * L + nt * 64 + 4 * i) * 2;
  const int voffA = (kg * D + mt0 * 80 + i) * 16;
  f32x4 acc[5][4];
#pragma unroll
  for (int a = 0; a < 5; ++a)
#pragma unroll
    for (int c = 0; c < 4; ++c) acc[a][c] = (f32x4){0.f, 0.f, 0.f, 0.f};
  if (wave < 2) gemm_body<14, L, false>(rsW, rsA, voffW, voffA, wave * 14, acc);
  else gemm_body<13, L, false>(rsW, rsA, voffW, voffA, 28 + (wave - 2) * 13, acc);
  f32x4 own[5];
#pragma unroll
  for (int c = 0; c < 4; ++c) {
#pragma unroll
    for (int mt = 0; mt < 5; ++mt) {
      if (c == wave) own[mt] = acc[mt][c];
      else *(f32x4*)(smem + ((c * 4 + wave) * 5 + mt) * 1024 + lane * 16) = acc[mt][c];
    }
  }
  const int s = nt * 64 + 4 * i + wave;
  const float bias = bf2f(b2[s]);
  const size_t eo = (size_t)s * D + mt0 * 80 + 4 * kg;
  u32x2 hr[5];
#pragma unroll
  for (int mt = 0; mt < 5; ++mt) hr[mt] = *(const u32x2*)(ws + OFF_H + (eo + mt * 16) * 2);
  __syncthreads();
#pragma unroll
  for (int o = 1; o < 4; ++o) {
    const int src = (wave + o) & 3;
#pragma unroll
    for (int mt = 0; mt < 5; ++mt) own[mt] += *(const f32x4*)(smem + ((wave * 4 + src) * 5 + mt) * 1024 + lane * 16);
  }
#pragma unroll
  for (int mt = 0; mt < 5; ++mt) {
    const u32 p0 = pk_bf16(own[mt][0], own[mt][1]), p1 = pk_bf16(own[mt][2], own[mt][3]);
    const u32 q0 = pk_bf16(bflo(p0) + bias, bfhi(p0) + bias), q1 = pk_bf16(bflo(p1) + bias, bfhi(p1) + bias);
    u32x2 o;
    o.x = pk_bf16(bflo(hr[mt].x) + bflo(q0), bfhi(hr[mt].x) + bfhi(q0));
    o.y = pk_bf16(bflo(hr[mt].y) + bflo(q1), bfhi(hr[mt].y) + bfhi(q1));
    *(u32x2*)(out + eo + mt * 16) = o;
  }
}

extern "C" void kernel_launch(void* const* d_in, const int* in_sizes, int n_in, void* d_out, int out_size, void* d_ws, size_t ws_size,
                              hipStream_t stream) {
  const u16* x = (const u16*)d_in[0];
  const u16* logA = (const u16*)d_in[1];
  const u16* Aim = (const u16*)d_in[2];
  const u16* Cre = (const u16*)d_in[3];
  const u16* Cim = (const u16*)d_in[4];
  const u16* logdt = (const u16*)d_in[5];
  const u16* dskip = (const u16*)d_in[6];
  const u16* w1 = (const u16*)d_in[8];
  const u16* b1 = (const u16*)d_in[9];
  const u16* w2 = (const u16*)d_in[10];
  const u16* b2 = (const u16*)d_in[11];
  char* ws = (char*)d_ws;
  (void)hipFuncSetAttribute((const void*)k2_conv, hipFuncAttributeMaxDynamicSharedMemorySize, 4 * CV_WAVE);
  (void)hipFuncSetAttribute((const void*)k4_gemm1, hipFuncAttributeMaxDynamicSharedMemorySize, 81920);
  (void)hipFuncSetAttribute((const void*)k5_gemm2, hipFuncAttributeMaxDynamicSharedMemorySize, 81920);
  k1_ln_kgen<<<256 + D, 128, 0, stream>>>(x, logA, Aim, Cre, Cim, logdt, ws);
  k2_conv<<<160, 256, 4 * CV_WAVE, stream>>>(ws);
  k3_ln2<<<256, 128, 0, stream>>>(x, dskip, ws);
  k4_gemm1<<<256, 256, 81920, stream>>>(w1, b1, ws);
  k5_gemm2<<<256, 256, 81920, stream>>>(w2, b2, ws, (u16*)d_out);
}
```

```cpp
#include <hip/hip_runtime.h>
#include <hip/hip_bf16.h>
#include <stdint.h>
#include <type_traits>

typedef unsigned short u16;
typedef unsigned int u32;
typedef u32 u32x2 __attribute__((ext_vector_type(2)));
typedef u32 u32x3 __attribute__((ext_vector_type(3)));
typedef u32 u32x4 __attribute__((ext_vector_type(4)));
typedef float f32x2 __attribute__((ext_vector_type(2)));
typedef float f32x4 __attribute__((ext_vector_type(4)));
typedef float f32x16 __attribute__((ext_vector_type(16)));
typedef u32 u32x16 __attribute__((ext_vector_type(16)));
typedef __bf16 bf16x2 __attribute__((ext_vector_type(2)));
typedef __bf16 bf16x4 __attribute__((ext_vector_type(4)));
typedef _Float16 f16x2 __attribute__((ext_vector_type(2)));
typedef _Float16 f16x8 __attribute__((ext_vector_type(8)));
typedef __amdgpu_buffer_rsrc_t rsrc_t;
#define LAS __attribute__((address_space(3)))
#define PIN() __builtin_amdgcn_sched_barrier(0)

constexpr int L = 2048, D = 640, NM = 128, F = 1728;
constexpr float EPS = 1e-5f;
constexpr int SPIN = 262144;

constexpr int SZ_ACT = L * D * 2;
constexpr int OFF_U = 4096;
constexpr int OFF_Y = OFF_U + 2 * SZ_ACT;
constexpr int YB = 10368;
constexpr int OFF_HN = OFF_Y + 256 * YB;
constexpr int OFF_G = OFF_HN + SZ_ACT;

constexpr int WPIECE = 1088, WSLOT = 4 * WPIECE, WREG = 40960, XTRA = 8 * WSLOT;
constexpr int KG_ROW = 528;
constexpr int CV_R = 4224, CV_U = 4640;
constexpr int U_OFF = WREG - 2 * CV_U, R_OFF = U_OFF - 2 * CV_R;
constexpr int TROW = 1344;
static_assert(53 * KG_ROW <= U_OFF && 5 * WSLOT <= R_OFF && R_OFF % 16 == 0, "LDS layout");
#define P0_ASM \
  "buffer_load_dwordx2 v[96:97], %[vx], %[rx], %[s0] offen offset:0\nbuffer_load_dwordx2 v[98:99], %[vx], %[rx], %[s0] offen offset:256\n" \
  "buffer_load_dwordx2 v[100:101], %[vx], %[rx], %[s0] offen offset:512\nbuffer_load_dwordx2 v[102:103], %[vx], %[rx], %[s0] offen offset:768\n" \
  "buffer_load_dwordx2 v[104:105], %[vx], %[rx], %[s0] offen offset:1024\ns_load_dword %[ep], %[ws], 0x0\n" \
  "s_cmp_ge_u32 %[bk], 160\ns_cbranch_scc1 1f\n" \
  "global_load_dword v106, %[v0], %[a]\nglobal_load_dword v107, %[v0], %[b]\n" \
  "global_load_dword v108, %[v0], %[c]\nglobal_load_dword v109, %[v0], %[d]\n" \
  "s_load_dwordx2 s[92:93], %[pdt], %[d2]\ns_load_dwordx2 s[94:95], %[pds], %[d2]\n" \
  "1:\n" \
  ""
#define P1_ASM \
  "s_waitcnt vmcnt(4) lgkmcnt(0)\nbuffer_load_dword %[pf], %[v1], %[rp], %[s] offen\n" \
  ""
#define IDLE_ASM \
  "s_add_u32 m0, %[l], 0\ns_add_u32 %[t], %[sw2], 1703936\n" \
  "s_movk_i32 %[k], 4\n1:\n" \
  "buffer_load_dwordx4 %[vw2], %[rw2], %[t] offen lds\ns_add_u32 m0, m0, 1088\n" \
  "s_add_u32 %[t], %[t], 32768\ns_sub_u32 %[k], %[k], 1\n" \
  "s_cmp_lg_u32 %[k], 0\ns_cbranch_scc1 1b\n" \
  "s_movk_i32 %[k], 96\nv_xad_u32 v56, %[xc], 0, %[rb]\n" \
  "v_xad_u32 v57, %[xc], 32, %[rb]\nv_xad_u32 v58, %[xc], 64, %[rb]\n" \
  "v_xad_u32 v59, %[xc], %[k], %[rb]\ns_mov_b32 %[k], 5\n" \
  "2:\ns_cmp_ge_u32 %[k], 14\n" \
  "s_cbranch_scc1 20f\ns_cmp_ge_u32 %[k], 5\n" \
  "s_cselect_b32 m0, -5, 9\ns_add_u32 m0, m0, %[k]\n" \
  "s_lshl_b32 m0, m0, 17\ns_add_u32 %[k], %[k], 1\n" \
  "buffer_load_dword v60, %[v2], %[rw2], m0 offen\ns_branch 2b\n" \
  "20:\ns_waitcnt vmcnt(9)\n" \
  "ds_read_b64_tr_b16 a[0:1], v56 offset:13056\nds_read_b64_tr_b16 a[2:3], v56 offset:13568\n" \
  "ds_read_b64_tr_b16 a[4:5], v57 offset:13056\nds_read_b64_tr_b16 a[6:7], v57 offset:13568\n" \
  "ds_read_b64_tr_b16 a[8:9], v58 offset:13056\nds_read_b64_tr_b16 a[10:11], v58 offset:13568\n" \
  "ds_read_b64_tr_b16 a[12:13], v59 offset:13056\nds_read_b64_tr_b16 a[14:15], v59 offset:13568\n" \
  "ds_read_b64_tr_b16 a[16:17], v56 offset:17408\nds_read_b64_tr_b16 a[18:19], v56 offset:17920\n" \
  "ds_read_b64_tr_b16 a[20:21], v57 offset:17408\nds_read_b64_tr_b16 a[22:23], v57 offset:17920\n" \
  "ds_read_b64_tr_b16 a[24:25], v58 offset:17408\nds_read_b64_tr_b16 a[26:27], v58 offset:17920\n" \
  "ds_read_b64_tr_b16 a[28:29], v59 offset:17408\nds_read_b64_tr_b16 a[30:31], v59 offset:17920\n" \
  "ds_read_b64_tr_b16 a[32:33], v56 offset:21760\nds_read_b64_tr_b16 a[34:35], v56 offset:22272\n" \
  "ds_read_b64_tr_b16 a[36:37], v57 offset:21760\nds_read_b64_tr_b16 a[38:39], v57 offset:22272\n" \
  "ds_read_b64_tr_b16 a[40:41], v58 offset:21760\nds_read_b64_tr_b16 a[42:43], v58 offset:22272\n" \
  "ds_read_b64_tr_b16 a[44:45], v59 offset:21760\nds_read_b64_tr_b16 a[46:47], v59 offset:22272\n" \
  "ds_read_b64_tr_b16 a[48:49], v56 offset:26112\nds_read_b64_tr_b16 a[50:51], v56 offset:26624\n" \
  "ds_read_b64_tr_b16 a[52:53], v57 offset:26112\nds_read_b64_tr_b16 a[54:55], v57 offset:26624\n" \
  "ds_read_b64_tr_b16 a[56:57], v58 offset:26112\nds_read_b64_tr_b16 a[58:59], v58 offset:26624\n" \
  "ds_read_b64_tr_b16 a[60:61], v59 offset:26112\nds_read_b64_tr_b16 a[62:63], v59 offset:26624\n" \
  "ds_read_b64_tr_b16 a[64:65], v56 offset:0\nds_read_b64_tr_b16 a[66:67], v56 offset:512\n" \
  "ds_read_b64_tr_b16 a[68:69], v57 offset:0\nds_read_b64_tr_b16 a[70:71], v57 offset:512\n" \
  "ds_read_b64_tr_b16 a[72:73], v58 offset:0\nds_read_b64_tr_b16 a[74:75], v58 offset:512\n" \
  "ds_read_b64_tr_b16 a[76:77], v59 offset:0\nds_read_b64_tr_b16 a[78:79], v59 offset:512\n" \
  "s_waitcnt lgkmcnt(0)\ns_barrier\n" \
  "s_add_u32 m0, %[l], 0\ns_add_u32 %[t], %[sw2], 0\n" \
  "s_movk_i32 %[k], 16\n3:\n" \
  "buffer_load_dwordx4 %[vw2], %[rw2], %[t] offen lds\ns_add_u32 m0, m0, 1088\n" \
  "s_add_u32 %[t], %[t], 32768\ns_sub_u32 %[k], %[k], 1\n" \
  "s_cmp_lg_u32 %[k], 0\ns_cbranch_scc1 3b\n" \
  "s_add_u32 m0, %[l], 34816\ns_add_u32 %[t], %[sw2], 524288\n" \
  "s_movk_i32 %[k], 4\n4:\n" \
  "buffer_load_dwordx4 %[vw2], %[rw2], %[t] offen lds\ns_add_u32 m0, m0, 1088\n" \
  "s_add_u32 %[t], %[t], 32768\ns_sub_u32 %[k], %[k], 1\n" \
  "s_cmp_lg_u32 %[k], 0\ns_cbranch_scc1 4b\n" \
  "s_waitcnt vmcnt(0)\n" \
  ""
#define CONV_ASM \
  "s_nop 4\nds_read_b64 v[96:97], %[tp0] offset:4032\n" \
  "ds_read_b64 v[98:99], %[tp0] offset:4040\nds_read_b128 v[128:131], %[up] offset:448\n" \
  "ds_read_b128 v[132:135], %[up] offset:960\nds_read_b128 v[136:139], %[up] offset:1472\n" \
  "ds_read_b128 v[140:143], %[up] offset:1984\nds_read_b128 v[144:147], %[up] offset:2496\n" \
  "ds_read_b128 v[148:151], %[up] offset:3008\nds_read_b128 v[152:155], %[up] offset:3520\n" \
  "ds_read_b128 v[156:159], %[up] offset:4032\nds_read_b64 v[100:101], %[tp0] offset:3520\n" \
  "ds_read_b64 v[102:103], %[tp0] offset:3528\nds_read_b64 v[104:105], %[tp0] offset:3008\n" \
  "ds_read_b64 v[106:107], %[tp0] offset:3016\nds_read_b64 v[108:109], %[tp0] offset:2496\n" \
  "ds_read_b64 v[110:111], %[tp0] offset:2504\nds_read_b64 v[112:113], %[tp0] offset:1984\n" \
  "ds_read_b64 v[114:115], %[tp0] offset:1992\nds_read_b64 v[116:117], %[tp0] offset:1472\n" \
  "ds_read_b64 v[118:119], %[tp0] offset:1480\nds_read_b64 v[120:121], %[tp0] offset:960\n" \
  "ds_read_b64 v[122:123], %[tp0] offset:968\nds_read_b64 v[124:125], %[tp0] offset:448\n" \
  "ds_read_b64 v[126:127], %[tp0] offset:456\ns_movk_i32 %[k], 96\n" \
  "v_xad_u32 v56, %[xc], 0, %[rb]\nv_xad_u32 v57, %[xc], 32, %[rb]\n" \
  "v_xad_u32 v58, %[xc], 64, %[rb]\nv_xad_u32 v59, %[xc], %[k], %[rb]\n" \
  "s_mov_b32 m0, %[l]\ns_add_u32 %[t], %[sw], 442368\n" \
  "v_mov_b32 v60, -1\nv_mov_b32 v61, -1\n" \
  "v_mov_b32 v62, -1\nv_mov_b32 v63, -1\n" \
  "s_waitcnt lgkmcnt(15)\nv_mfma_f32_16x16x32_bf16 v[64:67], v[96:99], v[128:131], 0\n" \
  "v_mfma_f32_16x16x32_bf16 v[68:71], v[96:99], v[132:135], 0\nv_mfma_f32_16x16x32_bf16 v[72:75], v[96:99], v[136:139], 0\n" \
  "v_mfma_f32_16x16x32_bf16 v[76:79], v[96:99], v[140:143], 0\nv_mfma_f32_16x16x32_bf16 v[80:83], v[96:99], v[144:147], 0\n" \
  "v_mfma_f32_16x16x32_bf16 v[84:87], v[96:99], v[148:151], 0\nv_mfma_f32_16x16x32_bf16 v[88:91], v[96:99], v[152:155], 0\n" \
  "s_waitcnt lgkmcnt(14)\nv_mfma_f32_16x16x32_bf16 v[92:95], v[96:99], v[156:159], 0\n" \
  "s_waitcnt lgkmcnt(12)\nv_mfma_f32_16x16x32_bf16 v[68:71], v[100:103], v[128:131], v[68:71]\n" \
  "ds_read_b64 v[160:161], %[tp0] offset:3968\nds_read_b64 v[162:163], %[tp0] offset:3976\n" \
  "v_mfma_f32_16x16x32_bf16 v[72:75], v[100:103], v[132:135], v[72:75]\nds_read_b128 v[192:195], %[up] offset:384\n" \
  "v_mfma_f32_16x16x32_bf16 v[76:79], v[100:103], v[136:139], v[76:79]\nbuffer_load_dwordx4 %[vw], %[rw], %[t] offen lds\n" \
  "s_add_u32 m0, m0, 1088\ns_add_u32 %[t], %[t], 27648\n" \
  "v_mfma_f32_16x16x32_bf16 v[80:83], v[100:103], v[140:143], v[80:83]\nds_read_b128 v[196:199], %[up] offset:896\n" \
  "v_mfma_f32_16x16x32_bf16 v[84:87], v[100:103], v[144:147], v[84:87]\nds_read_b128 v[200:203], %[up] offset:1408\n" \
  "v_mfma_f32_16x16x32_bf16 v[88:91], v[100:103], v[148:151], v[88:91]\nds_read_b128 v[204:207], %[up] offset:1920\n" \
  "v_mfma_f32_16x16x32_bf16 v[92:95], v[100:103], v[152:155], v[92:95]\nbuffer_load_dwordx4 %[vw], %[rw], %[t] offen lds\n" \
  "s_add_u32 m0, m0, 1088\ns_add_u32 %[t], %[t], 27648\n" \
  "s_waitcnt lgkmcnt(15)\nv_mfma_f32_16x16x32_bf16 v[72:75], v[104:107], v[128:131], v[72:75]\n" \
  "ds_read_b128 v[208:211], %[up] offset:2432\nv_mfma_f32_16x16x32_bf16 v[76:79], v[104:107], v[132:135], v[76:79]\n" \
  "ds_read_b128 v[212:215], %[up] offset:2944\nv_mfma_f32_16x16x32_bf16 v[80:83], v[104:107], v[136:139], v[80:83]\n" \
  "ds_read_b128 v[216:219], %[up] offset:3456\nv_mfma_f32_16x16x32_bf16 v[84:87], v[104:107], v[140:143], v[84:87]\n" \
  "buffer_load_dwordx4 %[vw], %[rw], %[t] offen lds\ns_add_u32 m0, m0, 1088\n" \
  "s_add_u32 %[t], %[t], 27648\nv_mfma_f32_16x16x32_bf16 v[88:91], v[104:107], v[144:147], v[88:91]\n" \
  "ds_read_b128 v[220:223], %[up] offset:3968\nv_mfma_f32_16x16x32_bf16 v[92:95], v[104:107], v[148:151], v[92:95]\n" \
  "ds_read_b64 v[164:165], %[tp0] offset:3456\nds_read_b64 v[166:167], %[tp0] offset:3464\n" \
  "s_waitcnt lgkmcnt(15)\nv_mfma_f32_16x16x32_bf16 v[76:79], v[108:111], v[128:131], v[76:79]\n" \
  "ds_read_b64 v[168:169], %[tp0] offset:2944\nds_read_b64 v[170:171], %[tp0] offset:2952\n" \
  "v_mfma_f32_16x16x32_bf16 v[80:83], v[108:111], v[132:135], v[80:83]\nbuffer_load_dwordx4 %[vw], %[rw], %[t] offen lds\n" \
  "s_add_u32 m0, m0, 1088\ns_add_u32 %[t], %[t], 27648\n" \
  "v_mfma_f32_16x16x32_bf16 v[84:87], v[108:111], v[136:139], v[84:87]\nds_read_b64 v[172:173], %[tp0] offset:2432\n" \
  "ds_read_b64 v[174:175], %[tp0] offset:2440\nv_mfma_f32_16x16x32_bf16 v[88:91], v[108:111], v[140:143], v[88:91]\n" \
  "ds_read_b64 v[176:177], %[tp0] offset:1920\nds_read_b64 v[178:179], %[tp0] offset:1928\n" \
  "v_mfma_f32_16x16x32_bf16 v[92:95], v[108:111], v[144:147], v[92:95]\nds_read_b64 v[180:181], %[tp0] offset:1408\n" \
  "ds_read_b64 v[182:183], %[tp0] offset:1416\nv_mfma_f32_16x16x32_bf16 v[80:83], v[112:115], v[128:131], v[80:83]\n" \
  "buffer_load_dwordx4 %[vw], %[rw], %[t] offen lds\ns_add_u32 m0, m0, 1088\n" \
  "s_add_u32 %[t], %[t], 27648\nv_mfma_f32_16x16x32_bf16 v[84:87], v[112:115], v[132:135], v[84:87]\n" \
  "ds_read_b64 v[184:185], %[tp0] offset:896\nds_read_b64 v[186:187], %[tp0] offset:904\n" \
  "v_mfma_f32_16x16x32_bf16 v[88:91], v[112:115], v[136:139], v[88:91]\nds_read_b64 v[188:189], %[tp0] offset:384\n" \
  "ds_read_b64 v[190:191], %[tp0] offset:392\nv_mfma_f32_16x16x32_bf16 v[92:95], v[112:115], v[140:143], v[92:95]\n" \
  "v_mfma_f32_16x16x32_bf16 v[84:87], v[116:119], v[128:131], v[84:87]\nbuffer_load_dwordx4 %[vw], %[rw], %[t] offen lds\n" \
  "s_add_u32 m0, m0, 1088\ns_add_u32 %[t], %[t], 27648\n" \
  "v_mfma_f32_16x16x32_bf16 v[88:91], v[116:119], v[132:135], v[88:91]\nv_mfma_f32_16x16x32_bf16 v[92:95], v[116:119], v[136:139], v[92:95]\n" \
  "s_waitcnt lgkmcnt(15)\nv_mfma_f32_16x16x32_bf16 v[88:91], v[120:123], v[128:131], v[88:91]\n" \
  "v_mfma_f32_16x16x32_bf16 v[92:95], v[120:123], v[132:135], v[92:95]\nbuffer_load_dwordx4 %[vw], %[rw], %[t] offen lds\n" \
  "s_add_u32 m0, m0, 1088\ns_add_u32 %[t], %[t], 27648\n" \
  "v_mfma_f32_16x16x32_bf16 v[92:95], v[124:127], v[128:131], v[92:95]\nv_mfma_f32_16x16x32_bf16 v[64:67], v[160:163], v[192:195], v[64:67]\n" \
  "ds_read_b64 v[96:97], %[tp0] offset:3904\nds_read_b64 v[98:99], %[tp0] offset:3912\n" \
  "v_mfma_f32_16x16x32_bf16 v[68:71], v[160:163], v[196:199], v[68:71]\nds_read_b128 v[128:131], %[up] offset:320\n" \
  "v_mfma_f32_16x16x32_bf16 v[72:75], v[160:163], v[200:203], v[72:75]\nbuffer_load_dwordx4 %[vw], %[rw], %[t] offen lds\n" \
  "s_add_u32 m0, m0, 1088\ns_add_u32 %[t], %[t], 27648\n" \
  "v_mfma_f32_16x16x32_bf16 v[76:79], v[160:163], v[204:207], v[76:79]\nds_read_b128 v[132:135], %[up] offset:832\n" \
  "v_mfma_f32_16x16x32_bf16 v[80:83], v[160:163], v[208:211], v[80:83]\nds_read_b128 v[136:139], %[up] offset:1344\n" \
  "v_mfma_f32_16x16x32_bf16 v[84:87], v[160:163], v[212:215], v[84:87]\nds_read_b128 v[140:143], %[up] offset:1856\n" \
  "v_mfma_f32_16x16x32_bf16 v[88:91], v[160:163], v[216:219], v[88:91]\nbuffer_load_dwordx4 %[vw], %[rw], %[t] offen lds\n" \
  "s_add_u32 m0, m0, 1088\ns_add_u32 %[t], %[t], 27648\n" \
  "s_waitcnt lgkmcnt(6)\nv_mfma_f32_16x16x32_bf16 v[92:95], v[160:163], v[220:223], v[92:95]\n" \
  "ds_read_b128 v[144:147], %[up] offset:2368\nv_mfma_f32_16x16x32_bf16 v[68:71], v[164:167], v[192:195], v[68:71]\n" \
  "ds_read_b128 v[148:151], %[up] offset:2880\nv_mfma_f32_16x16x32_bf16 v[72:75], v[164:167], v[196:199], v[72:75]\n" \
  "ds_read_b128 v[152:155], %[up] offset:3392\nv_mfma_f32_16x16x32_bf16 v[76:79], v[164:167], v[200:203], v[76:79]\n" \
  "ds_read_b128 v[156:159], %[up] offset:3904\nv_mfma_f32_16x16x32_bf16 v[80:83], v[164:167], v[204:207], v[80:83]\n" \
  "buffer_load_dwordx4 %[vw], %[rw], %[t] offen lds\ns_add_u32 m0, m0, 1088\n" \
  "s_add_u32 %[t], %[t], 27648\nv_mfma_f32_16x16x32_bf16 v[84:87], v[164:167], v[208:211], v[84:87]\n" \
  "ds_read_b64 v[100:101], %[tp0] offset:3392\nds_read_b64 v[102:103], %[tp0] offset:3400\n" \
  "v_mfma_f32_16x16x32_bf16 v[88:91], v[164:167], v[212:215], v[88:91]\nds_read_b64 v[104:105], %[tp0] offset:2880\n" \
  "ds_read_b64 v[106:107], %[tp0] offset:2888\nv_mfma_f32_16x16x32_bf16 v[92:95], v[164:167], v[216:219], v[92:95]\n" \
  "ds_read_b64 v[108:109], %[tp0] offset:2368\nds_read_b64 v[110:111], %[tp0] offset:2376\n" \
  "v_mfma_f32_16x16x32_bf16 v[72:75], v[168:171], v[192:195], v[72:75]\nbuffer_load_dwordx4 %[vw], %[rw], %[t] offen lds\n" \
  "s_add_u32 m0, m0, 1088\ns_add_u32 %[t], %[t], 27648\n" \
  "v_mfma_f32_16x16x32_bf16 v[76:79], v[168:171], v[196:199], v[76:79]\nds_read_b64 v[112:113], %[tp0] offset:1856\n" \
  "ds_read_b64 v[114:115], %[tp0] offset:1864\nv_mfma_f32_16x16x32_bf16 v[80:83], v[168:171], v[200:203], v[80:83]\n" \
  "ds_read_b64 v[116:117], %[tp0] offset:1344\nds_read_b64 v[118:119], %[tp0] offset:1352\n" \
  "v_mfma_f32_16x16x32_bf16 v[84:87], v[168:171], v[204:207], v[84:87]\nds_read_b64 v[120:121], %[tp0] offset:832\n" \
  "ds_read_b64 v[122:123], %[tp0] offset:840\nv_mfma_f32_16x16x32_bf16 v[88:91], v[168:171], v[208:211], v[88:91]\n" \
  "ds_read_b64 v[124:125], %[tp0] offset:320\nds_read_b64 v[126:127], %[tp0] offset:328\n" \
  "v_mfma_f32_16x16x32_bf16 v[92:95], v[168:171], v[212:215], v[92:95]\nbuffer_load_dwordx4 %[vw], %[rw], %[t] offen lds\n" \
  "s_add_u32 m0, m0, 1088\ns_add_u32 %[t], %[t], 27648\n" \
  "v_mfma_f32_16x16x32_bf16 v[76:79], v[172:175], v[192:195], v[76:79]\nv_mfma_f32_16x16x32_bf16 v[80:83], v[172:175], v[196:199], v[80:83]\n" \
  "v_mfma_f32_16x16x32_bf16 v[84:87], v[172:175], v[200:203], v[84:87]\nv_mfma_f32_16x16x32_bf16 v[88:91], v[172:175], v[204:207], v[88:91]\n" \
  "buffer_load_dwordx4 %[vw], %[rw], %[t] offen lds\ns_add_u32 m0, m0, 1088\n" \
  "s_add_u32 %[t], %[t], 27648\nv_mfma_f32_16x16x32_bf16 v[92:95], v[172:175], v[208:211], v[92:95]\n" \
  "v_mfma_f32_16x16x32_bf16 v[80:83], v[176:179], v[192:195], v[80:83]\nv_mfma_f32_16x16x32_bf16 v[84:87], v[176:179], v[196:199], v[84:87]\n" \
  "v_mfma_f32_16x16x32_bf16 v[88:91], v[176:179], v[200:203], v[88:91]\nv_mfma_f32_16x16x32_bf16 v[92:95], v[176:179], v[204:207], v[92:95]\n" \
  "buffer_load_dwordx4 %[vw], %[rw], %[t] offen lds\ns_add_u32 m0, m0, 1088\n" \
  "s_add_u32 %[t], %[t], 27648\nv_mfma_f32_16x16x32_bf16 v[84:87], v[180:183], v[192:195], v[84:87]\n" \
  "v_mfma_f32_16x16x32_bf16 v[88:91], v[180:183], v[196:199], v[88:91]\nv_mfma_f32_16x16x32_bf16 v[92:95], v[180:183], v[200:203], v[92:95]\n" \
  "v_mfma_f32_16x16x32_bf16 v[88:91], v[184:187], v[192:195], v[88:91]\nbuffer_load_dwordx4 %[vw], %[rw], %[t] offen lds\n" \
  "s_add_u32 m0, m0, 1088\ns_add_u32 %[t], %[t], 27648\n" \
  "v_mfma_f32_16x16x32_bf16 v[92:95], v[184:187], v[196:199], v[92:95]\nv_mfma_f32_16x16x32_bf16 v[92:95], v[188:191], v[192:195], v[92:95]\n" \
  "s_waitcnt lgkmcnt(0)\nv_mfma_f32_16x16x32_bf16 v[64:67], v[96:99], v[128:131], v[64:67]\n" \
  "ds_read_b64 v[160:161], %[tp0] offset:3840\nds_read_b64 v[162:163], %[tp0] offset:3848\n" \
  "v_mfma_f32_16x16x32_bf16 v[68:71], v[96:99], v[132:135], v[68:71]\nds_read_b128 v[192:195], %[up] offset:256\n" \
  "v_mfma_f32_16x16x32_bf16 v[72:75], v[96:99], v[136:139], v[72:75]\nbuffer_load_dwordx4 %[vw], %[rw], %[t] offen lds\n" \
  "s_add_u32 m0, m0, 1088\ns_add_u32 %[t], %[t], 27648\n" \
  "v_mfma_f32_16x16x32_bf16 v[76:79], v[96:99], v[140:143], v[76:79]\nds_read_b128 v[196:199], %[up] offset:768\n" \
  "v_mfma_f32_16x16x32_bf16 v[80:83], v[96:99], v[144:147], v[80:83]\nds_read_b128 v[200:203], %[up] offset:1280\n" \
  "v_mfma_f32_16x16x32_bf16 v[84:87], v[96:99], v[148:151], v[84:87]\nds_read_b128 v[204:207], %[up] offset:1792\n" \
  "v_mfma_f32_16x16x32_bf16 v[88:91], v[96:99], v[152:155], v[88:91]\nbuffer_load_dwordx4 %[vw], %[rw], %[t] offen lds\n" \
  "s_add_u32 m0, m0, 1088\ns_add_u32 %[t], %[t], 27648\n" \
  "v_mfma_f32_16x16x32_bf16 v[92:95], v[96:99], v[156:159], v[92:95]\nds_read_b128 v[208:211], %[up] offset:2304\n" \
  "v_mfma_f32_16x16x32_bf16 v[68:71], v[100:103], v[128:131], v[68:71]\nds_read_b128 v[212:215], %[up] offset:2816\n" \
  "v_mfma_f32_16x16x32_bf16 v[72:75], v[100:103], v[132:135], v[72:75]\nds_read_b128 v[216:219], %[up] offset:3328\n" \
  "v_mfma_f32_16x16x32_bf16 v[76:79], v[100:103], v[136:139], v[76:79]\nds_read_b128 v[220:223], %[up] offset:3840\n" \
  "v_mfma_f32_16x16x32_bf16 v[80:83], v[100:103], v[140:143], v[80:83]\nbuffer_load_dwordx4 %[vw], %[rw], %[t] offen lds\n" \
  "s_add_u32 m0, m0, 1088\ns_add_u32 %[t], %[t], 27648\n" \
  "v_mfma_f32_16x16x32_bf16 v[84:87], v[100:103], v[144:147], v[84:87]\nds_read_b64 v[164:165], %[tp0] offset:3328\n" \
  "ds_read_b64 v[166:167], %[tp0] offset:3336\nv_mfma_f32_16x16x32_bf16 v[88:91], v[100:103], v[148:151], v[88:91]\n" \
  "ds_read_b64 v[168:169], %[tp0] offset:2816\nds_read_b64 v[170:171], %[tp0] offset:2824\n" \
  "v_mfma_f32_16x16x32_bf16 v[92:95], v[100:103], v[152:155], v[92:95]\nds_read_b64 v[172:173], %[tp0] offset:2304\n" \
  "ds_read_b64 v[174:175], %[tp0] offset:2312\nv_mfma_f32_16x16x32_bf16 v[72:75], v[104:107], v[128:131], v[72:75]\n" \
  "buffer_load_dwordx4 %[vw], %[rw], %[t] offen lds\ns_add_u32 m0, m0, 1088\n" \
  "s_add_u32 %[t], %[t], 27648\nv_mfma_f32_16x16x32_bf16 v[76:79], v[104:107], v[132:135], v[76:79]\n" \
  "ds_read_b64 v[176:177], %[tp0] offset:1792\nds_read_b64 v[178:179], %[tp0] offset:1800\n" \
  "v_mfma_f32_16x16x32_bf16 v[80:83], v[104:107], v[136:139], v[80:83]\nds_read_b64 v[180:181], %[tp0] offset:1280\n" \
  "ds_read_b64 v[182:183], %[tp0] offset:1288\nv_mfma_f32_16x16x32_bf16 v[84:87], v[104:107], v[140:143], v[84:87]\n" \
  "ds_read_b64 v[184:185], %[tp0] offset:768\nds_read_b64 v[186:187], %[tp0] offset:776\n" \
  "v_mfma_f32_16x16x32_bf16 v[88:91], v[104:107], v[144:147], v[88:91]\nds_read_b64 v[188:189], %[tp0] offset:256\n" \
  "ds_read_b64 v[190:191], %[tp0] offset:264\nv_mfma_f32_16x16x32_bf16 v[92:95], v[104:107], v[148:151], v[92:95]\n" \
  "buffer_load_dwordx4 %[vw], %[rw], %[t] offen lds\ns_add_u32 m0, m0, 1088\n" \
  "s_add_u32 %[t], %[t], 27648\ns_mov_b32 m0, %[l]\n" \
  "v_mfma_f32_16x16x32_bf16 v[76:79], v[108:111], v[128:131], v[76:79]\nv_mfma_f32_16x16x32_bf16 v[80:83], v[108:111], v[132:135], v[80:83]\n" \
  "v_mfma_f32_16x16x32_bf16 v[84:87], v[108:111], v[136:139], v[84:87]\nv_mfma_f32_16x16x32_bf16 v[88:91], v[108:111], v[140:143], v[88:91]\n" \
  "buffer_store_dwordx4 v[60:63], %[vh], %[rh], %[sh] offen offset:0 sc1\nv_mfma_f32_16x16x32_bf16 v[92:95], v[108:111], v[144:147], v[92:95]\n" \
  "v_mfma_f32_16x16x32_bf16 v[80:83], v[112:115], v[128:131], v[80:83]\nv_mfma_f32_16x16x32_bf16 v[84:87], v[112:115], v[132:135], v[84:87]\n" \
  "v_mfma_f32_16x16x32_bf16 v[88:91], v[112:115], v[136:139], v[88:91]\nv_mfma_f32_16x16x32_bf16 v[92:95], v[112:115], v[140:143], v[92:95]\n" \
  "buffer_store_dwordx4 v[60:63], %[vh], %[rh], %[sh] offen offset:1024 sc1\nv_mfma_f32_16x16x32_bf16 v[84:87], v[116:119], v[128:131], v[84:87]\n" \
  "v_mfma_f32_16x16x32_bf16 v[88:91], v[116:119], v[132:135], v[88:91]\nv_mfma_f32_16x16x32_bf16 v[92:95], v[116:119], v[136:139], v[92:95]\n" \
  "v_mfma_f32_16x16x32_bf16 v[88:91], v[120:123], v[128:131], v[88:91]\nbuffer_store_dwordx4 v[60:63], %[vh], %[rh], %[sh] offen offset:2048 sc1\n" \
  "v_mfma_f32_16x16x32_bf16 v[92:95], v[120:123], v[132:135], v[92:95]\nv_mfma_f32_16x16x32_bf16 v[92:95], v[124:127], v[128:131], v[92:95]\n" \
  "s_waitcnt lgkmcnt(0)\nv_mfma_f32_16x16x32_bf16 v[64:67], v[160:163], v[192:195], v[64:67]\n" \
  "ds_read_b64 v[96:97], %[tp0] offset:3776\nds_read_b64 v[98:99], %[tp0] offset:3784\n" \
  "v_mfma_f32_16x16x32_bf16 v[68:71], v[160:163], v[196:199], v[68:71]\nds_read_b128 v[128:131], %[up] offset:192\n" \
  "v_mfma_f32_16x16x32_bf16 v[72:75], v[160:163], v[200:203], v[72:75]\nds_read_b128 v[132:135], %[up] offset:704\n" \
  "v_mfma_f32_16x16x32_bf16 v[76:79], v[160:163], v[204:207], v[76:79]\nbuffer_store_dwordx4 v[60:63], %[vh], %[rh], %[sh] offen offset:3072 sc1\n" \
  "v_mfma_f32_16x16x32_bf16 v[80:83], v[160:163], v[208:211], v[80:83]\nds_read_b128 v[136:139], %[up] offset:1216\n" \
  "v_mfma_f32_16x16x32_bf16 v[84:87], v[160:163], v[212:215], v[84:87]\nds_read_b128 v[140:143], %[up] offset:1728\n" \
  "v_mfma_f32_16x16x32_bf16 v[88:91], v[160:163], v[216:219], v[88:91]\nds_read_b128 v[144:147], %[up] offset:2240\n" \
  "v_mfma_f32_16x16x32_bf16 v[92:95], v[160:163], v[220:223], v[92:95]\nds_read_b128 v[148:151], %[up] offset:2752\n" \
  "v_mfma_f32_16x16x32_bf16 v[68:71], v[164:167], v[192:195], v[68:71]\nds_read_b128 v[152:155], %[up] offset:3264\n" \
  "v_mfma_f32_16x16x32_bf16 v[72:75], v[164:167], v[196:199], v[72:75]\nds_read_b128 v[156:159], %[up] offset:3776\n" \
  "v_mfma_f32_16x16x32_bf16 v[76:79], v[164:167], v[200:203], v[76:79]\ns_add_u32 %[k], %[sh], 4096\n" \
  "buffer_store_dwordx4 v[60:63], %[vh], %[rh], %[k] offen offset:0 sc1\nv_mfma_f32_16x16x32_bf16 v[80:83], v[164:167], v[204:207], v[80:83]\n" \
  "ds_read_b64 v[100:101], %[tp0] offset:3264\nds_read_b64 v[102:103], %[tp0] offset:3272\n" \
  "v_mfma_f32_16x16x32_bf16 v[84:87], v[164:167], v[208:211], v[84:87]\nds_read_b64 v[104:105], %[tp0] offset:2752\n" \
  "ds_read_b64 v[106:107], %[tp0] offset:2760\nv_mfma_f32_16x16x32_bf16 v[88:91], v[164:167], v[212:215], v[88:91]\n" \
  "ds_read_b64 v[108:109], %[tp0] offset:2240\nds_read_b64 v[110:111], %[tp0] offset:2248\n" \
  "v_mfma_f32_16x16x32_bf16 v[92:95], v[164:167], v[216:219], v[92:95]\nds_read_b64 v[112:113], %[tp0] offset:1728\n" \
  "ds_read_b64 v[114:115], %[tp0] offset:1736\nv_mfma_f32_16x16x32_bf16 v[72:75], v[168:171], v[192:195], v[72:75]\n" \
  "ds_read_b64 v[116:117], %[tp0] offset:1216\nds_read_b64 v[118:119], %[tp0] offset:1224\n" \
  "v_mfma_f32_16x16x32_bf16 v[76:79], v[168:171], v[196:199], v[76:79]\nds_read_b64 v[120:121], %[tp0] offset:704\n" \
  "ds_read_b64 v[122:123], %[tp0] offset:712\nv_mfma_f32_16x16x32_bf16 v[80:83], v[168:171], v[200:203], v[80:83]\n" \
  "ds_read_b64 v[124:125], %[tp0] offset:192\nds_read_b64 v[126:127], %[tp0] offset:200\n" \
  "v_mfma_f32_16x16x32_bf16 v[84:87], v[168:171], v[204:207], v[84:87]\nbuffer_store_dwordx4 v[60:63], %[vh], %[rh], %[k] offen offset:1024 sc1\n" \
  "v_mfma_f32_16x16x32_bf16 v[88:91], v[168:171], v[208:211], v[88:91]\nv_mfma_f32_16x16x32_bf16 v[92:95], v[168:171], v[212:215], v[92:95]\n" \
  "v_mfma_f32_16x16x32_bf16 v[76:79], v[172:175], v[192:195], v[76:79]\nv_mfma_f32_16x16x32_bf16 v[80:83], v[172:175], v[196:199], v[80:83]\n" \
  "v_mfma_f32_16x16x32_bf16 v[84:87], v[172:175], v[200:203], v[84:87]\nv_mfma_f32_16x16x32_bf16 v[88:91], v[172:175], v[204:207], v[88:91]\n" \
  "v_mfma_f32_16x16x32_bf16 v[92:95], v[172:175], v[208:211], v[92:95]\nbuffer_store_dwordx4 v[60:63], %[vh], %[rh], %[k] offen offset:2048 sc1\n" \
  "v_mfma_f32_16x16x32_bf16 v[80:83], v[176:179], v[192:195], v[80:83]\nv_mfma_f32_16x16x32_bf16 v[84:87], v[176:179], v[196:199], v[84:87]\n" \
  "v_mfma_f32_16x16x32_bf16 v[88:91], v[176:179], v[200:203], v[88:91]\nv_mfma_f32_16x16x32_bf16 v[92:95], v[176:179], v[204:207], v[92:95]\n" \
  "v_mfma_f32_16x16x32_bf16 v[84:87], v[180:183], v[192:195], v[84:87]\nv_mfma_f32_16x16x32_bf16 v[88:91], v[180:183], v[196:199], v[88:91]\n" \
  "v_mfma_f32_16x16x32_bf16 v[92:95], v[180:183], v[200:203], v[92:95]\nbuffer_store_dwordx4 v[60:63], %[vh], %[rh], %[k] offen offset:3072 sc1\n" \
  "v_mfma_f32_16x16x32_bf16 v[88:91], v[184:187], v[192:195], v[88:91]\nv_mfma_f32_16x16x32_bf16 v[92:95], v[184:187], v[196:199], v[92:95]\n" \
  "v_mfma_f32_16x16x32_bf16 v[92:95], v[188:191], v[192:195], v[92:95]\ns_waitcnt vmcnt(8)\n" \
  "s_waitcnt lgkmcnt(0)\nv_mfma_f32_16x16x32_bf16 v[64:67], v[96:99], v[128:131], v[64:67]\n" \
  "ds_read_b64 v[160:161], %[tp0] offset:3712\nds_read_b64 v[162:163], %[tp0] offset:3720\n" \
  "v_mfma_f32_16x16x32_bf16 v[68:71], v[96:99], v[132:135], v[68:71]\nds_read_b64_tr_b16 a[0:1], v56 offset:0\n" \
  "v_mfma_f32_16x16x32_bf16 v[72:75], v[96:99], v[136:139], v[72:75]\nds_read_b128 v[192:195], %[up] offset:128\n" \
  "v_mfma_f32_16x16x32_bf16 v[76:79], v[96:99], v[140:143], v[76:79]\nds_read_b64_tr_b16 a[2:3], v56 offset:512\n" \
  "v_mfma_f32_16x16x32_bf16 v[80:83], v[96:99], v[144:147], v[80:83]\nds_read_b128 v[196:199], %[up] offset:640\n" \
  "v_mfma_f32_16x16x32_bf16 v[84:87], v[96:99], v[148:151], v[84:87]\nds_read_b64_tr_b16 a[4:5], v57 offset:0\n" \
  "v_mfma_f32_16x16x32_bf16 v[88:91], v[96:99], v[152:155], v[88:91]\nds_read_b128 v[200:203], %[up] offset:1152\n" \
  "v_mfma_f32_16x16x32_bf16 v[92:95], v[96:99], v[156:159], v[92:95]\nds_read_b64_tr_b16 a[6:7], v57 offset:512\n" \
  "v_mfma_f32_16x16x32_bf16 v[68:71], v[100:103], v[128:131], v[68:71]\nds_read_b128 v[204:207], %[up] offset:1664\n" \
  "v_mfma_f32_16x16x32_bf16 v[72:75], v[100:103], v[132:135], v[72:75]\nds_read_b64_tr_b16 a[8:9], v58 offset:0\n" \
  "v_mfma_f32_16x16x32_bf16 v[76:79], v[100:103], v[136:139], v[76:79]\nds_read_b128 v[208:211], %[up] offset:2176\n" \
  "v_mfma_f32_16x16x32_bf16 v[80:83], v[100:103], v[140:143], v[80:83]\nds_read_b64_tr_b16 a[10:11], v58 offset:512\n" \
  "v_mfma_f32_16x16x32_bf16 v[84:87], v[100:103], v[144:147], v[84:87]\nds_read_b128 v[212:215], %[up] offset:2688\n" \
  "v_mfma_f32_16x16x32_bf16 v[88:91], v[100:103], v[148:151], v[88:91]\nds_read_b64_tr_b16 a[12:13], v59 offset:0\n" \
  "v_mfma_f32_16x16x32_bf16 v[92:95], v[100:103], v[152:155], v[92:95]\nds_read_b128 v[216:219], %[up] offset:3200\n" \
  "v_mfma_f32_16x16x32_bf16 v[72:75], v[104:107], v[128:131], v[72:75]\nds_read_b64_tr_b16 a[14:15], v59 offset:512\n" \
  "v_mfma_f32_16x16x32_bf16 v[76:79], v[104:107], v[132:135], v[76:79]\nds_read_b128 v[220:223], %[up] offset:3712\n" \
  "v_mfma_f32_16x16x32_bf16 v[80:83], v[104:107], v[136:139], v[80:83]\nds_read_b64_tr_b16 a[16:17], v56 offset:4352\n" \
  "v_mfma_f32_16x16x32_bf16 v[84:87], v[104:107], v[140:143], v[84:87]\nds_read_b64 v[164:165], %[tp0] offset:3200\n" \
  "ds_read_b64 v[166:167], %[tp0] offset:3208\nv_mfma_f32_16x16x32_bf16 v[88:91], v[104:107], v[144:147], v[88:91]\n" \
  "ds_read_b64_tr_b16 a[18:19], v56 offset:4864\nv_mfma_f32_16x16x32_bf16 v[92:95], v[104:107], v[148:151], v[92:95]\n" \
  "ds_read_b64 v[168:169], %[tp0] offset:2688\nds_read_b64 v[170:171], %[tp0] offset:2696\n" \
  "v_mfma_f32_16x16x32_bf16 v[76:79], v[108:111], v[128:131], v[76:79]\nds_read_b64_tr_b16 a[20:21], v57 offset:4352\n" \
  "v_mfma_f32_16x16x32_bf16 v[80:83], v[108:111], v[132:135], v[80:83]\nds_read_b64 v[172:173], %[tp0] offset:2176\n" \
  "ds_read_b64 v[174:175], %[tp0] offset:2184\nv_mfma_f32_16x16x32_bf16 v[84:87], v[108:111], v[136:139], v[84:87]\n" \
  "ds_read_b64_tr_b16 a[22:23], v57 offset:4864\nv_mfma_f32_16x16x32_bf16 v[88:91], v[108:111], v[140:143], v[88:91]\n" \
  "ds_read_b64 v[176:177], %[tp0] offset:1664\nds_read_b64 v[178:179], %[tp0] offset:1672\n" \
  "v_mfma_f32_16x16x32_bf16 v[92:95], v[108:111], v[144:147], v[92:95]\nds_read_b64_tr_b16 a[24:25], v58 offset:4352\n" \
  "v_mfma_f32_16x16x32_bf16 v[80:83], v[112:115], v[128:131], v[80:83]\nds_read_b64 v[180:181], %[tp0] offset:1152\n" \
  "ds_read_b64 v[182:183], %[tp0] offset:1160\nv_mfma_f32_16x16x32_bf16 v[84:87], v[112:115], v[132:135], v[84:87]\n" \
  "ds_read_b64_tr_b16 a[26:27], v58 offset:4864\nv_mfma_f32_16x16x32_bf16 v[88:91], v[112:115], v[136:139], v[88:91]\n" \
  "ds_read_b64 v[184:185], %[tp0] offset:640\nds_read_b64 v[186:187], %[tp0] offset:648\n" \
  "v_mfma_f32_16x16x32_bf16 v[92:95], v[112:115], v[140:143], v[92:95]\nds_read_b64_tr_b16 a[28:29], v59 offset:4352\n" \
  "v_mfma_f32_16x16x32_bf16 v[84:87], v[116:119], v[128:131], v[84:87]\nds_read_b64 v[188:189], %[tp0] offset:128\n" \
  "ds_read_b64 v[190:191], %[tp0] offset:136\nv_mfma_f32_16x16x32_bf16 v[88:91], v[116:119], v[132:135], v[88:91]\n" \
  "ds_read_b64_tr_b16 a[30:31], v59 offset:4864\nv_mfma_f32_16x16x32_bf16 v[92:95], v[116:119], v[136:139], v[92:95]\n" \
  "v_mfma_f32_16x16x32_bf16 v[88:91], v[120:123], v[128:131], v[88:91]\nv_mfma_f32_16x16x32_bf16 v[92:95], v[120:123], v[132:135], v[92:95]\n" \
  "v_mfma_f32_16x16x32_bf16 v[92:95], v[124:127], v[128:131], v[92:95]\ns_waitcnt lgkmcnt(0)\n" \
  "v_mfma_f32_16x16x32_bf16 v[64:67], v[160:163], v[192:195], v[64:67]\nds_read_b64 v[96:97], %[tp0] offset:3648\n" \
  "ds_read_b64 v[98:99], %[tp0] offset:3656\nv_mfma_f32_16x16x32_bf16 v[68:71], v[160:163], v[196:199], v[68:71]\n" \
  "ds_read_b64_tr_b16 a[32:33], v56 offset:8704\nv_mfma_f32_16x16x32_bf16 v[72:75], v[160:163], v[200:203], v[72:75]\n" \
  "ds_read_b128 v[128:131], %[up] offset:64\nv_mfma_f32_16x16x32_bf16 v[76:79], v[160:163], v[204:207], v[76:79]\n" \
  "ds_read_b64_tr_b16 a[34:35], v56 offset:9216\nv_mfma_f32_16x16x32_bf16 v[80:83], v[160:163], v[208:211], v[80:83]\n" \
  "ds_read_b128 v[132:135], %[up] offset:576\nv_mfma_f32_16x16x32_bf16 v[84:87], v[160:163], v[212:215], v[84:87]\n" \
  "buffer_load_dwordx4 %[vw], %[rw], %[t] offen lds\ns_add_u32 m0, m0, 1088\n" \
  "s_add_u32 %[t], %[t], 27648\nv_mfma_f32_16x16x32_bf16 v[88:91], v[160:163], v[216:219], v[88:91]\n" \
  "ds_read_b64_tr_b16 a[36:37], v57 offset:8704\nv_mfma_f32_16x16x32_bf16 v[92:95], v[160:163], v[220:223], v[92:95]\n" \
  "ds_read_b128 v[136:139], %[up] offset:1088\nv_mfma_f32_16x16x32_bf16 v[68:71], v[164:167], v[192:195], v[68:71]\n" \
  "ds_read_b64_tr_b16 a[38:39], v57 offset:9216\nv_mfma_f32_16x16x32_bf16 v[72:75], v[164:167], v[196:199], v[72:75]\n" \
  "ds_read_b128 v[140:143], %[up] offset:1600\nv_mfma_f32_16x16x32_bf16 v[76:79], v[164:167], v[200:203], v[76:79]\n" \
  "ds_read_b64_tr_b16 a[40:41], v58 offset:8704\nv_mfma_f32_16x16x32_bf16 v[80:83], v[164:167], v[204:207], v[80:83]\n" \
  "buffer_load_dwordx4 %[vw], %[rw], %[t] offen lds\ns_add_u32 m0, m0, 1088\n" \
  "s_add_u32 %[t], %[t], 27648\nv_mfma_f32_16x16x32_bf16 v[84:87], v[164:167], v[208:211], v[84:87]\n" \
  "ds_read_b128 v[144:147], %[up] offset:2112\nv_mfma_f32_16x16x32_bf16 v[88:91], v[164:167], v[212:215], v[88:91]\n" \
  "ds_read_b64_tr_b16 a[42:43], v58 offset:9216\nv_mfma_f32_16x16x32_bf16 v[92:95], v[164:167], v[216:219], v[92:95]\n" \
  "ds_read_b128 v[148:151], %[up] offset:2624\nv_mfma_f32_16x16x32_bf16 v[72:75], v[168:171], v[192:195], v[72:75]\n" \
  "ds_read_b64_tr_b16 a[44:45], v59 offset:8704\nv_mfma_f32_16x16x32_bf16 v[76:79], v[168:171], v[196:199], v[76:79]\n" \
  "ds_read_b128 v[152:155], %[up] offset:3136\nv_mfma_f32_16x16x32_bf16 v[80:83], v[168:171], v[200:203], v[80:83]\n" \
  "buffer_load_dwordx4 %[vw], %[rw], %[t] offen lds\ns_add_u32 m0, m0, 1088\n" \
  "s_add_u32 %[t], %[t], 27648\nv_mfma_f32_16x16x32_bf16 v[84:87], v[168:171], v[204:207], v[84:87]\n" \
  "ds_read_b64_tr_b16 a[46:47], v59 offset:9216\nv_mfma_f32_16x16x32_bf16 v[88:91], v[168:171], v[208:211], v[88:91]\n" \
  "ds_read_b128 v[156:159], %[up] offset:3648\nv_mfma_f32_16x16x32_bf16 v[92:95], v[168:171], v[212:215], v[92:95]\n" \
  "ds_read_b64_tr_b16 a[48:49], v56 offset:13056\nv_mfma_f32_16x16x32_bf16 v[76:79], v[172:175], v[192:195], v[76:79]\n" \
  "ds_read_b64 v[100:101], %[tp0] offset:3136\nds_read_b64 v[102:103], %[tp0] offset:3144\n" \
  "v_mfma_f32_16x16x32_bf16 v[80:83], v[172:175], v[196:199], v[80:83]\nds_read_b64_tr_b16 a[50:51], v56 offset:13568\n" \
  "v_mfma_f32_16x16x32_bf16 v[84:87], v[172:175], v[200:203], v[84:87]\nbuffer_load_dwordx4 %[vw], %[rw], %[t] offen lds\n" \
  "s_add_u32 m0, m0, 1088\ns_add_u32 %[t], %[t], 27648\n" \
  "v_mfma_f32_16x16x32_bf16 v[88:91], v[172:175], v[204:207], v[88:91]\nds_read_b64 v[104:105], %[tp0] offset:2624\n" \
  "ds_read_b64 v[106:107], %[tp0] offset:2632\nv_mfma_f32_16x16x32_bf16 v[92:95], v[172:175], v[208:211], v[92:95]\n" \
  "ds_read_b64_tr_b16 a[52:53], v57 offset:13056\nv_mfma_f32_16x16x32_bf16 v[80:83], v[176:179], v[192:195], v[80:83]\n" \
  "ds_read_b64 v[108:109], %[tp0] offset:2112\nds_read_b64 v[110:111], %[tp0] offset:2120\n" \
  "v_mfma_f32_16x16x32_bf16 v[84:87], v[176:179], v[196:199], v[84:87]\nds_read_b64_tr_b16 a[54:55], v57 offset:13568\n" \
  "v_mfma_f32_16x16x32_bf16 v[88:91], v[176:179], v[200:203], v[88:91]\nds_read_b64 v[112:113], %[tp0] offset:1600\n" \
  "ds_read_b64 v[114:115], %[tp0] offset:1608\nv_mfma_f32_16x16x32_bf16 v[92:95], v[176:179], v[204:207], v[92:95]\n" \
  "ds_read_b64 v[116:117], %[tp0] offset:1088\nds_read_b64 v[118:119], %[tp0] offset:1096\n" \
  "v_mfma_f32_16x16x32_bf16 v[84:87], v[180:183], v[192:195], v[84:87]\nds_read_b64 v[120:121], %[tp0] offset:576\n" \
  "ds_read_b64 v[122:123], %[tp0] offset:584\nv_mfma_f32_16x16x32_bf16 v[88:91], v[180:183], v[196:199], v[88:91]\n" \
  "ds_read_b64 v[124:125], %[tp0] offset:64\nds_read_b64 v[126:127], %[tp0] offset:72\n" \
  "v_mfma_f32_16x16x32_bf16 v[92:95], v[180:183], v[200:203], v[92:95]\nbuffer_load_dwordx4 %[vw], %[rw], %[t] offen lds\n" \
  "s_add_u32 m0, m0, 1088\ns_add_u32 %[t], %[t], 27648\n" \
  "v_mfma_f32_16x16x32_bf16 v[88:91], v[184:187], v[192:195], v[88:91]\nbuffer_load_dwordx4 %[vw], %[rw], %[t] offen lds\n" \
  "s_add_u32 m0, m0, 1088\ns_add_u32 %[t], %[t], 27648\n" \
  "v_mfma_f32_16x16x32_bf16 v[92:95], v[184:187], v[196:199], v[92:95]\nbuffer_load_dwordx4 %[vw], %[rw], %[t] offen lds\n" \
  "s_add_u32 m0, m0, 1088\ns_add_u32 %[t], %[t], 27648\n" \
  "v_mfma_f32_16x16x32_bf16 v[92:95], v[188:191], v[192:195], v[92:95]\nbuffer_load_dwordx4 %[vw], %[rw], %[t] offen lds\n" \
  "s_add_u32 m0, m0, 1088\ns_add_u32 %[t], %[t], 27648\n" \
  "s_waitcnt lgkmcnt(0)\nv_mfma_f32_16x16x32_bf16 v[64:67], v[96:99], v[128:131], v[64:67]\n" \
  "ds_read_b64 v[160:161], %[tp0] offset:3584\nds_read_b64 v[162:163], %[tp0] offset:3592\n" \
  "v_mfma_f32_16x16x32_bf16 v[68:71], v[96:99], v[132:135], v[68:71]\nds_read_b64_tr_b16 a[56:57], v58 offset:13056\n" \
  "v_mfma_f32_16x16x32_bf16 v[72:75], v[96:99], v[136:139], v[72:75]\nds_read_b128 v[192:195], %[up] offset:0\n" \
  "v_mfma_f32_16x16x32_bf16 v[76:79], v[96:99], v[140:143], v[76:79]\nds_read_b64_tr_b16 a[58:59], v58 offset:13568\n" \
  "v_mfma_f32_16x16x32_bf16 v[80:83], v[96:99], v[144:147], v[80:83]\nds_read_b128 v[196:199], %[up] offset:512\n" \
  "v_mfma_f32_16x16x32_bf16 v[84:87], v[96:99], v[148:151], v[84:87]\nds_read_b64_tr_b16 a[60:61], v59 offset:13056\n" \
  "v_mfma_f32_16x16x32_bf16 v[88:91], v[96:99], v[152:155], v[88:91]\nds_read_b128 v[200:203], %[up] offset:1024\n" \
  "v_mfma_f32_16x16x32_bf16 v[92:95], v[96:99], v[156:159], v[92:95]\nds_read_b64_tr_b16 a[62:63], v59 offset:13568\n" \
  "v_mfma_f32_16x16x32_bf16 v[68:71], v[100:103], v[128:131], v[68:71]\nds_read_b128 v[204:207], %[up] offset:1536\n" \
  "v_mfma_f32_16x16x32_bf16 v[72:75], v[100:103], v[132:135], v[72:75]\nds_read_b64_tr_b16 a[64:65], v56 offset:17408\n" \
  "v_mfma_f32_16x16x32_bf16 v[76:79], v[100:103], v[136:139], v[76:79]\nds_read_b128 v[208:211], %[up] offset:2048\n" \
  "v_mfma_f32_16x16x32_bf16 v[80:83], v[100:103], v[140:143], v[80:83]\nds_read_b64_tr_b16 a[66:67], v56 offset:17920\n" \
  "v_mfma_f32_16x16x32_bf16 v[84:87], v[100:103], v[144:147], v[84:87]\nds_read_b128 v[212:215], %[up] offset:2560\n" \
  "v_mfma_f32_16x16x32_bf16 v[88:91], v[100:103], v[148:151], v[88:91]\nds_read_b64_tr_b16 a[68:69], v57 offset:17408\n" \
  "v_mfma_f32_16x16x32_bf16 v[92:95], v[100:103], v[152:155], v[92:95]\nds_read_b128 v[216:219], %[up] offset:3072\n" \
  "v_mfma_f32_16x16x32_bf16 v[72:75], v[104:107], v[128:131], v[72:75]\nds_read_b64_tr_b16 a[70:71], v57 offset:17920\n" \
  "v_mfma_f32_16x16x32_bf16 v[76:79], v[104:107], v[132:135], v[76:79]\nds_read_b128 v[220:223], %[up] offset:3584\n" \
  "v_mfma_f32_16x16x32_bf16 v[80:83], v[104:107], v[136:139], v[80:83]\nds_read_b64_tr_b16 a[72:73], v58 offset:17408\n" \
  "v_mfma_f32_16x16x32_bf16 v[84:87], v[104:107], v[140:143], v[84:87]\nds_read_b64 v[164:165], %[tp0] offset:3072\n" \
  "ds_read_b64 v[166:167], %[tp0] offset:3080\nv_mfma_f32_16x16x32_bf16 v[88:91], v[104:107], v[144:147], v[88:91]\n" \
  "ds_read_b64_tr_b16 a[74:75], v58 offset:17920\nv_mfma_f32_16x16x32_bf16 v[92:95], v[104:107], v[148:151], v[92:95]\n" \
  "ds_read_b64 v[168:169], %[tp0] offset:2560\nds_read_b64 v[170:171], %[tp0] offset:2568\n" \
  "v_mfma_f32_16x16x32_bf16 v[76:79], v[108:111], v[128:131], v[76:79]\nds_read_b64_tr_b16 a[76:77], v59 offset:17408\n" \
  "v_mfma_f32_16x16x32_bf16 v[80:83], v[108:111], v[132:135], v[80:83]\nds_read_b64 v[172:173], %[tp0] offset:2048\n" \
  "ds_read_b64 v[174:175], %[tp0] offset:2056\nv_mfma_f32_16x16x32_bf16 v[84:87], v[108:111], v[136:139], v[84:87]\n" \
  "ds_read_b64_tr_b16 a[78:79], v59 offset:17920\nv_mfma_f32_16x16x32_bf16 v[88:91], v[108:111], v[140:143], v[88:91]\n" \
  "ds_read_b64 v[176:177], %[tp0] offset:1536\nds_read_b64 v[178:179], %[tp0] offset:1544\n" \
  "v_mfma_f32_16x16x32_bf16 v[92:95], v[108:111], v[144:147], v[92:95]\nds_read_b64 v[180:181], %[tp0] offset:1024\n" \
  "ds_read_b64 v[182:183], %[tp0] offset:1032\nv_mfma_f32_16x16x32_bf16 v[80:83], v[112:115], v[128:131], v[80:83]\n" \
  "ds_read_b64 v[184:185], %[tp0] offset:512\nds_read_b64 v[186:187], %[tp0] offset:520\n" \
  "v_mfma_f32_16x16x32_bf16 v[84:87], v[112:115], v[132:135], v[84:87]\nds_read_b64 v[188:189], %[tp0] offset:0\n" \
  "ds_read_b64 v[190:191], %[tp0] offset:8\nv_mfma_f32_16x16x32_bf16 v[88:91], v[112:115], v[136:139], v[88:91]\n" \
  "v_mfma_f32_16x16x32_bf16 v[92:95], v[112:115], v[140:143], v[92:95]\nv_mfma_f32_16x16x32_bf16 v[84:87], v[116:119], v[128:131], v[84:87]\n" \
  "v_mfma_f32_16x16x32_bf16 v[88:91], v[116:119], v[132:135], v[88:91]\nv_mfma_f32_16x16x32_bf16 v[92:95], v[116:119], v[136:139], v[92:95]\n" \
  "buffer_load_dwordx4 %[vw], %[rw], %[t] offen lds\ns_add_u32 m0, m0, 1088\n" \
  "s_add_u32 %[t], %[t], 27648\nv_mfma_f32_16x16x32_bf16 v[88:91], v[120:123], v[128:131], v[88:91]\n" \
  "buffer_load_dwordx4 %[vw], %[rw], %[t] offen lds\ns_add_u32 m0, m0, 1088\n" \
  "s_add_u32 %[t], %[t], 27648\nv_mfma_f32_16x16x32_bf16 v[92:95], v[120:123], v[132:135], v[92:95]\n" \
  "buffer_load_dwordx4 %[vw], %[rw], %[t] offen lds\ns_add_u32 m0, m0, 1088\n" \
  "s_add_u32 %[t], %[t], 27648\nv_mfma_f32_16x16x32_bf16 v[92:95], v[124:127], v[128:131], v[92:95]\n" \
  "buffer_load_dwordx4 %[vw], %[rw], %[t] offen lds\ns_add_u32 m0, m0, 1088\n" \
  "s_add_u32 %[t], %[t], 27648\ns_waitcnt lgkmcnt(0)\n" \
  "v_mfma_f32_16x16x32_bf16 v[64:67], v[160:163], v[192:195], v[64:67]\nv_mfma_f32_16x16x32_bf16 v[68:71], v[160:163], v[196:199], v[68:71]\n" \
  "v_mfma_f32_16x16x32_bf16 v[72:75], v[160:163], v[200:203], v[72:75]\nbuffer_load_dwordx4 %[vw], %[rw], %[t] offen lds\n" \
  "s_add_u32 m0, m0, 1088\ns_add_u32 %[t], %[t], 27648\n" \
  "v_mfma_f32_16x16x32_bf16 v[76:79], v[160:163], v[204:207], v[76:79]\nv_mfma_f32_16x16x32_bf16 v[80:83], v[160:163], v[208:211], v[80:83]\n" \
  "v_mfma_f32_16x16x32_bf16 v[84:87], v[160:163], v[212:215], v[84:87]\nv_mfma_f32_16x16x32_bf16 v[88:91], v[160:163], v[216:219], v[88:91]\n" \
  "buffer_load_dwordx4 %[vw], %[rw], %[t] offen lds\ns_add_u32 m0, m0, 1088\n" \
  "s_add_u32 %[t], %[t], 27648\nv_mfma_f32_16x16x32_bf16 v[92:95], v[160:163], v[220:223], v[92:95]\n" \
  "v_mfma_f32_16x16x32_bf16 v[68:71], v[164:167], v[192:195], v[68:71]\nv_mfma_f32_16x16x32_bf16 v[72:75], v[164:167], v[196:199], v[72:75]\n" \
  "v_mfma_f32_16x16x32_bf16 v[76:79], v[164:167], v[200:203], v[76:79]\nv_mfma_f32_16x16x32_bf16 v[80:83], v[164:167], v[204:207], v[80:83]\n" \
  "buffer_load_dwordx4 %[vw], %[rw], %[t] offen lds\ns_add_u32 m0, m0, 1088\n" \
  "s_add_u32 %[t], %[t], 27648\nv_mfma_f32_16x16x32_bf16 v[84:87], v[164:167], v[208:211], v[84:87]\n" \
  "v_mfma_f32_16x16x32_bf16 v[88:91], v[164:167], v[212:215], v[88:91]\nv_mfma_f32_16x16x32_bf16 v[92:95], v[164:167], v[216:219], v[92:95]\n" \
  "v_mfma_f32_16x16x32_bf16 v[72:75], v[168:171], v[192:195], v[72:75]\nbuffer_load_dwordx4 %[vw], %[rw], %[t] offen lds\n" \
  "s_add_u32 m0, m0, 1088\ns_add_u32 %[t], %[t], 27648\n" \
  "v_mfma_f32_16x16x32_bf16 v[76:79], v[168:171], v[196:199], v[76:79]\nv_mfma_f32_16x16x32_bf16 v[80:83], v[168:171], v[200:203], v[80:83]\n" \
  "v_mfma_f32_16x16x32_bf16 v[84:87], v[168:171], v[204:207], v[84:87]\nv_mfma_f32_16x16x32_bf16 v[88:91], v[168:171], v[208:211], v[88:91]\n" \
  "v_mfma_f32_16x16x32_bf16 v[92:95], v[168:171], v[212:215], v[92:95]\nbuffer_load_dwordx4 %[vw], %[rw], %[t] offen lds\n" \
  "s_add_u32 m0, m0, 1088\ns_add_u32 %[t], %[t], 27648\n" \
  "v_mfma_f32_16x16x32_bf16 v[76:79], v[172:175], v[192:195], v[76:79]\nv_mfma_f32_16x16x32_bf16 v[80:83], v[172:175], v[196:199], v[80:83]\n" \
  "v_mfma_f32_16x16x32_bf16 v[84:87], v[172:175], v[200:203], v[84:87]\nv_mfma_f32_16x16x32_bf16 v[88:91], v[172:175], v[204:207], v[88:91]\n" \
  "buffer_load_dwordx4 %[vw], %[rw], %[t] offen lds\ns_add_u32 m0, m0, 1088\n" \
  "s_add_u32 %[t], %[t], 27648\nv_mfma_f32_16x16x32_bf16 v[92:95], v[172:175], v[208:211], v[92:95]\n" \
  "v_mfma_f32_16x16x32_bf16 v[80:83], v[176:179], v[192:195], v[80:83]\nv_mfma_f32_16x16x32_bf16 v[84:87], v[176:179], v[196:199], v[84:87]\n" \
  "v_mfma_f32_16x16x32_bf16 v[88:91], v[176:179], v[200:203], v[88:91]\nv_mfma_f32_16x16x32_bf16 v[92:95], v[176:179], v[204:207], v[92:95]\n" \
  "buffer_load_dwordx4 %[vw], %[rw], %[t] offen lds\ns_add_u32 m0, m0, 1088\n" \
  "s_add_u32 %[t], %[t], 27648\nv_mfma_f32_16x16x32_bf16 v[84:87], v[180:183], v[192:195], v[84:87]\n" \
  "v_mfma_f32_16x16x32_bf16 v[88:91], v[180:183], v[196:199], v[88:91]\nv_mfma_f32_16x16x32_bf16 v[92:95], v[180:183], v[200:203], v[92:95]\n" \
  "v_mfma_f32_16x16x32_bf16 v[88:91], v[184:187], v[192:195], v[88:91]\nbuffer_load_dwordx4 %[vw], %[rw], %[t] offen lds\n" \
  "s_add_u32 m0, m0, 1088\ns_add_u32 %[t], %[t], 27648\n" \
  "v_mfma_f32_16x16x32_bf16 v[92:95], v[184:187], v[196:199], v[92:95]\nv_mfma_f32_16x16x32_bf16 v[92:95], v[188:191], v[192:195], v[92:95]\n" \
  "s_waitcnt vmcnt(20)\ns_nop 7\n" \
  ""
#define M2_ASM \
  "s_movk_i32 %[k], 96\nv_xad_u32 v56, %[xc], 0, %[rb]\n" \
  "v_xad_u32 v57, %[xc], 32, %[rb]\nv_xad_u32 v58, %[xc], 64, %[rb]\n" \
  "v_xad_u32 v59, %[xc], %[k], %[rb]\nds_read_b64_tr_b16 a[160:161], v56 offset:21760\n" \
  "ds_read_b64_tr_b16 a[162:163], v56 offset:22272\nds_read_b64_tr_b16 a[164:165], v57 offset:21760\n" \
  "ds_read_b64_tr_b16 a[166:167], v57 offset:22272\nds_read_b64_tr_b16 a[168:169], v58 offset:21760\n" \
  "ds_read_b64_tr_b16 a[170:171], v58 offset:22272\nds_read_b64_tr_b16 a[172:173], v59 offset:21760\n" \
  "ds_read_b64_tr_b16 a[174:175], v59 offset:22272\nds_read_b64_tr_b16 a[176:177], v56 offset:26112\n" \
  "ds_read_b64_tr_b16 a[178:179], v56 offset:26624\nds_read_b64_tr_b16 a[180:181], v57 offset:26112\n" \
  "ds_read_b64_tr_b16 a[182:183], v57 offset:26624\nds_read_b64_tr_b16 a[184:185], v58 offset:26112\n" \
  "ds_read_b64_tr_b16 a[186:187], v58 offset:26624\nds_read_b64_tr_b16 a[188:189], v59 offset:26112\n" \
  "ds_read_b64_tr_b16 a[190:191], v59 offset:26624\n" \
  ""
constexpr int NW = 20, NL2 = 4, PF0 = 1, CPG = 8, CPL = 72, SWP = 1, PW = 5, ROT = 0, JE = 14, RG = 2, OAUX = 17;
constexpr uint64_t CNT = 0x1414141414141414ull, BASE = 0x8c7864503c281400ull;
#define M1_ASM \
  "s_add_u32 m0, %[l], 21760\ns_add_u32 %[t], %[sw], 1548288\n" \
  "s_movk_i32 %[k], 8\n1:\n" \
  "buffer_load_dwordx4 %[vw], %[rw], %[t] offen lds\ns_add_u32 m0, m0, 1088\n" \
  "s_add_u32 %[t], %[t], 27648\ns_sub_u32 %[k], %[k], 1\n" \
  "s_cmp_lg_u32 %[k], 0\ns_cbranch_scc1 1b\n" \
  "s_add_u32 m0, %[l], 30464\ns_add_u32 %[t], %[sw], 0\n" \
  "s_movk_i32 %[k], 4\n2:\n" \
  "buffer_load_dwordx4 %[vw], %[rw], %[t] offen lds\ns_add_u32 m0, m0, 1088\n" \
  "s_add_u32 %[t], %[t], 27648\ns_sub_u32 %[k], %[k], 1\n" \
  "s_cmp_lg_u32 %[k], 0\ns_cbranch_scc1 2b\n" \
  "buffer_load_dwordx2 v[96:97], %[vx], %[rx], %[sx] offen offset:0\nbuffer_load_dwordx2 v[98:99], %[vx], %[rx], %[sx] offen offset:256\n" \
  "buffer_load_dwordx2 v[100:101], %[vx], %[rx], %[sx] offen offset:512\nbuffer_load_dwordx2 v[102:103], %[vx], %[rx], %[sx] offen offset:768\n" \
  "buffer_load_dwordx2 v[104:105], %[vx], %[rx], %[sx] offen offset:1024\ns_movk_i32 %[k], 96\n" \
  "v_xad_u32 v56, %[xc], 0, %[rb]\nv_xad_u32 v57, %[xc], 32, %[rb]\n" \
  "v_xad_u32 v58, %[xc], 64, %[rb]\nv_xad_u32 v59, %[xc], %[k], %[rb]\n" \
  "s_waitcnt vmcnt(21)\nds_read_b64_tr_b16 a[80:81], v56 offset:0\n" \
  "ds_read_b64_tr_b16 a[82:83], v56 offset:512\nds_read_b64_tr_b16 a[84:85], v57 offset:0\n" \
  "ds_read_b64_tr_b16 a[86:87], v57 offset:512\nds_read_b64_tr_b16 a[88:89], v58 offset:0\n" \
  "ds_read_b64_tr_b16 a[90:91], v58 offset:512\nds_read_b64_tr_b16 a[92:93], v59 offset:0\n" \
  "ds_read_b64_tr_b16 a[94:95], v59 offset:512\nds_read_b64_tr_b16 a[96:97], v56 offset:4352\n" \
  "ds_read_b64_tr_b16 a[98:99], v56 offset:4864\nds_read_b64_tr_b16 a[100:101], v57 offset:4352\n" \
  "ds_read_b64_tr_b16 a[102:103], v57 offset:4864\nds_read_b64_tr_b16 a[104:105], v58 offset:4352\n" \
  "ds_read_b64_tr_b16 a[106:107], v58 offset:4864\nds_read_b64_tr_b16 a[108:109], v59 offset:4352\n" \
  "ds_read_b64_tr_b16 a[110:111], v59 offset:4864\nds_read_b64_tr_b16 a[112:113], v56 offset:8704\n" \
  "ds_read_b64_tr_b16 a[114:115], v56 offset:9216\nds_read_b64_tr_b16 a[116:117], v57 offset:8704\n" \
  "ds_read_b64_tr_b16 a[118:119], v57 offset:9216\nds_read_b64_tr_b16 a[120:121], v58 offset:8704\n" \
  "ds_read_b64_tr_b16 a[122:123], v58 offset:9216\nds_read_b64_tr_b16 a[124:125], v59 offset:8704\n" \
  "ds_read_b64_tr_b16 a[126:127], v59 offset:9216\nds_read_b64_tr_b16 a[128:129], v56 offset:13056\n" \
  "ds_read_b64_tr_b16 a[130:131], v56 offset:13568\nds_read_b64_tr_b16 a[132:133], v57 offset:13056\n" \
  "ds_read_b64_tr_b16 a[134:135], v57 offset:13568\nds_read_b64_tr_b16 a[136:137], v58 offset:13056\n" \
  "ds_read_b64_tr_b16 a[138:139], v58 offset:13568\nds_read_b64_tr_b16 a[140:141], v59 offset:13056\n" \
  "ds_read_b64_tr_b16 a[142:143], v59 offset:13568\nds_read_b64_tr_b16 a[144:145], v56 offset:17408\n" \
  "ds_read_b64_tr_b16 a[146:147], v56 offset:17920\nds_read_b64_tr_b16 a[148:149], v57 offset:17408\n" \
  "ds_read_b64_tr_b16 a[150:151], v57 offset:17920\nds_read_b64_tr_b16 a[152:153], v58 offset:17408\n" \
  "ds_read_b64_tr_b16 a[154:155], v58 offset:17920\nds_read_b64_tr_b16 a[156:157], v59 offset:17408\n" \
  "ds_read_b64_tr_b16 a[158:159], v59 offset:17920\ns_waitcnt vmcnt(17)\n" \
  "s_barrier\nbuffer_load_dwordx2 v[106:107], %[vy], %[ry], %[sy] offen offset:0 sc1\n" \
  "buffer_load_dwordx2 v[108:109], %[vy], %[ry], %[sy] offen offset:2048 sc1\ns_add_u32 %[t], %[sy], 4096\n" \
  "buffer_load_dwordx2 v[110:111], %[vy], %[ry], %[t] offen offset:0 sc1\nbuffer_load_dwordx2 v[112:113], %[vy], %[ry], %[t] offen offset:2048 sc1\n" \
  "s_add_u32 %[t], %[sy], 8192\nbuffer_load_dwordx2 v[114:115], %[vy], %[ry], %[t] offen offset:0 sc1\n" \
  "s_waitcnt lgkmcnt(0)\ns_add_u32 m0, %[l], 0\n" \
  "s_add_u32 %[t], %[sw], 110592\ns_movk_i32 %[k], 8\n" \
  "3:\nbuffer_load_dwordx4 %[vw], %[rw], %[t] offen lds\n" \
  "s_add_u32 m0, m0, 1088\ns_add_u32 %[t], %[t], 27648\n" \
  "s_sub_u32 %[k], %[k], 1\ns_cmp_lg_u32 %[k], 0\n" \
  "s_cbranch_scc1 3b\ns_waitcnt vmcnt(8) lgkmcnt(0)\n" \
  ""
#define HELP_ASM \
  "buffer_load_dword v60, %[v1], %[rp], %[ja] offen\ns_add_u32 m0, %[l], 0\n" \
  "s_add_u32 %[t], %[sw], 442368\ns_movk_i32 %[k], 20\n" \
  "1:\nbuffer_load_dwordx4 %[vw], %[rw], %[t] offen lds\n" \
  "s_add_u32 m0, m0, 1088\ns_add_u32 %[t], %[t], 27648\n" \
  "s_sub_u32 %[k], %[k], 1\ns_cmp_lg_u32 %[k], 0\n" \
  "s_cbranch_scc1 1b\ns_movk_i32 %[k], 96\n" \
  "v_xad_u32 v56, %[xc], 0, %[rb]\nv_xad_u32 v57, %[xc], 32, %[rb]\n" \
  "v_xad_u32 v58, %[xc], 64, %[rb]\nv_xad_u32 v59, %[xc], %[k], %[rb]\n" \
  "s_waitcnt vmcnt(0)\nds_read_b64_tr_b16 a[0:1], v56 offset:0\n" \
  "ds_read_b64_tr_b16 a[2:3], v56 offset:512\nds_read_b64_tr_b16 a[4:5], v57 offset:0\n" \
  "ds_read_b64_tr_b16 a[6:7], v57 offset:512\nds_read_b64_tr_b16 a[8:9], v58 offset:0\n" \
  "ds_read_b64_tr_b16 a[10:11], v58 offset:512\nds_read_b64_tr_b16 a[12:13], v59 offset:0\n" \
  "ds_read_b64_tr_b16 a[14:15], v59 offset:512\nds_read_b64_tr_b16 a[16:17], v56 offset:4352\n" \
  "ds_read_b64_tr_b16 a[18:19], v56 offset:4864\nds_read_b64_tr_b16 a[20:21], v57 offset:4352\n" \
  "ds_read_b64_tr_b16 a[22:23], v57 offset:4864\nds_read_b64_tr_b16 a[24:25], v58 offset:4352\n" \
  "ds_read_b64_tr_b16 a[26:27], v58 offset:4864\nds_read_b64_tr_b16 a[28:29], v59 offset:4352\n" \
  "ds_read_b64_tr_b16 a[30:31], v59 offset:4864\nds_read_b64_tr_b16 a[32:33], v56 offset:8704\n" \
  "ds_read_b64_tr_b16 a[34:35], v56 offset:9216\nds_read_b64_tr_b16 a[36:37], v57 offset:8704\n" \
  "ds_read_b64_tr_b16 a[38:39], v57 offset:9216\nds_read_b64_tr_b16 a[40:41], v58 offset:8704\n" \
  "ds_read_b64_tr_b16 a[42:43], v58 offset:9216\nds_read_b64_tr_b16 a[44:45], v59 offset:8704\n" \
  "ds_read_b64_tr_b16 a[46:47], v59 offset:9216\nds_read_b64_tr_b16 a[48:49], v56 offset:13056\n" \
  "ds_read_b64_tr_b16 a[50:51], v56 offset:13568\nds_read_b64_tr_b16 a[52:53], v57 offset:13056\n" \
  "ds_read_b64_tr_b16 a[54:55], v57 offset:13568\nds_read_b64_tr_b16 a[56:57], v58 offset:13056\n" \
  "ds_read_b64_tr_b16 a[58:59], v58 offset:13568\nds_read_b64_tr_b16 a[60:61], v59 offset:13056\n" \
  "ds_read_b64_tr_b16 a[62:63], v59 offset:13568\nds_read_b64_tr_b16 a[64:65], v56 offset:17408\n" \
  "ds_read_b64_tr_b16 a[66:67], v56 offset:17920\nds_read_b64_tr_b16 a[68:69], v57 offset:17408\n" \
  "ds_read_b64_tr_b16 a[70:71], v57 offset:17920\nds_read_b64_tr_b16 a[72:73], v58 offset:17408\n" \
  "ds_read_b64_tr_b16 a[74:75], v58 offset:17920\nds_read_b64_tr_b16 a[76:77], v59 offset:17408\n" \
  "ds_read_b64_tr_b16 a[78:79], v59 offset:17920\ns_add_u32 %[t], %[hq], 12\n" \
  "11:\ns_add_u32 %[t], %[t], %[nh]\n" \
  "s_cmp_ge_u32 %[t], 20\ns_cbranch_scc1 12f\n" \
  "s_and_b32 m0, %[t], 15\ns_mul_i32 m0, m0, 110592\n" \
  "buffer_load_dword v60, %[v1], %[rp], m0 offen\ns_branch 11b\n" \
  "12:\ns_waitcnt lgkmcnt(0)\n" \
  "s_add_u32 m0, %[l], 0\ns_add_u32 %[t], %[sw], 995328\n" \
  "s_movk_i32 %[k], 28\n4:\n" \
  "buffer_load_dwordx4 %[vw], %[rw], %[t] offen lds\ns_add_u32 m0, m0, 1088\n" \
  "s_add_u32 %[t], %[t], 27648\ns_sub_u32 %[k], %[k], 1\n" \
  "s_cmp_lg_u32 %[k], 0\ns_cbranch_scc1 4b\n" \
  "s_add_u32 m0, %[l], 30464\ns_add_u32 %[t], %[sw], 0\n" \
  "s_movk_i32 %[k], 4\n3:\n" \
  "buffer_load_dwordx4 %[vw], %[rw], %[t] offen lds\ns_add_u32 m0, m0, 1088\n" \
  "s_add_u32 %[t], %[t], 27648\ns_sub_u32 %[k], %[k], 1\n" \
  "s_cmp_lg_u32 %[k], 0\ns_cbranch_scc1 3b\n" \
  "s_waitcnt vmcnt(12)\nds_read_b64_tr_b16 a[80:81], v56 offset:0\n" \
  "ds_read_b64_tr_b16 a[82:83], v56 offset:512\nds_read_b64_tr_b16 a[84:85], v57 offset:0\n" \
  "ds_read_b64_tr_b16 a[86:87], v57 offset:512\nds_read_b64_tr_b16 a[88:89], v58 offset:0\n" \
  "ds_read_b64_tr_b16 a[90:91], v58 offset:512\nds_read_b64_tr_b16 a[92:93], v59 offset:0\n" \
  "ds_read_b64_tr_b16 a[94:95], v59 offset:512\nds_read_b64_tr_b16 a[96:97], v56 offset:4352\n" \
  "ds_read_b64_tr_b16 a[98:99], v56 offset:4864\nds_read_b64_tr_b16 a[100:101], v57 offset:4352\n" \
  "ds_read_b64_tr_b16 a[102:103], v57 offset:4864\nds_read_b64_tr_b16 a[104:105], v58 offset:4352\n" \
  "ds_read_b64_tr_b16 a[106:107], v58 offset:4864\nds_read_b64_tr_b16 a[108:109], v59 offset:4352\n" \
  "ds_read_b64_tr_b16 a[110:111], v59 offset:4864\nds_read_b64_tr_b16 a[112:113], v56 offset:8704\n" \
  "ds_read_b64_tr_b16 a[114:115], v56 offset:9216\nds_read_b64_tr_b16 a[116:117], v57 offset:8704\n" \
  "ds_read_b64_tr_b16 a[118:119], v57 offset:9216\nds_read_b64_tr_b16 a[120:121], v58 offset:8704\n" \
  "ds_read_b64_tr_b16 a[122:123], v58 offset:9216\nds_read_b64_tr_b16 a[124:125], v59 offset:8704\n" \
  "ds_read_b64_tr_b16 a[126:127], v59 offset:9216\nds_read_b64_tr_b16 a[128:129], v56 offset:13056\n" \
  "ds_read_b64_tr_b16 a[130:131], v56 offset:13568\nds_read_b64_tr_b16 a[132:133], v57 offset:13056\n" \
  "ds_read_b64_tr_b16 a[134:135], v57 offset:13568\nds_read_b64_tr_b16 a[136:137], v58 offset:13056\n" \
  "ds_read_b64_tr_b16 a[138:139], v58 offset:13568\nds_read_b64_tr_b16 a[140:141], v59 offset:13056\n" \
  "ds_read_b64_tr_b16 a[142:143], v59 offset:13568\nds_read_b64_tr_b16 a[144:145], v56 offset:17408\n" \
  "ds_read_b64_tr_b16 a[146:147], v56 offset:17920\nds_read_b64_tr_b16 a[148:149], v57 offset:17408\n" \
  "ds_read_b64_tr_b16 a[150:151], v57 offset:17920\nds_read_b64_tr_b16 a[152:153], v58 offset:17408\n" \
  "ds_read_b64_tr_b16 a[154:155], v58 offset:17920\nds_read_b64_tr_b16 a[156:157], v59 offset:17408\n" \
  "ds_read_b64_tr_b16 a[158:159], v59 offset:17920\ns_waitcnt lgkmcnt(0)\n" \
  "s_add_u32 m0, %[l], 0\ns_add_u32 %[t], %[sw], 110592\n" \
  "s_movk_i32 %[k], 12\n5:\n" \
  "buffer_load_dwordx4 %[vw], %[rw], %[t] offen lds\ns_add_u32 m0, m0, 1088\n" \
  "s_add_u32 %[t], %[t], 27648\ns_sub_u32 %[k], %[k], 1\n" \
  "s_cmp_lg_u32 %[k], 0\ns_cbranch_scc1 5b\n" \
  "s_waitcnt vmcnt(16)\nds_read_b64_tr_b16 a[160:161], v56 offset:21760\n" \
  "ds_read_b64_tr_b16 a[162:163], v56 offset:22272\nds_read_b64_tr_b16 a[164:165], v57 offset:21760\n" \
  "ds_read_b64_tr_b16 a[166:167], v57 offset:22272\nds_read_b64_tr_b16 a[168:169], v58 offset:21760\n" \
  "ds_read_b64_tr_b16 a[170:171], v58 offset:22272\nds_read_b64_tr_b16 a[172:173], v59 offset:21760\n" \
  "ds_read_b64_tr_b16 a[174:175], v59 offset:22272\nds_read_b64_tr_b16 a[176:177], v56 offset:26112\n" \
  "ds_read_b64_tr_b16 a[178:179], v56 offset:26624\nds_read_b64_tr_b16 a[180:181], v57 offset:26112\n" \
  "ds_read_b64_tr_b16 a[182:183], v57 offset:26624\nds_read_b64_tr_b16 a[184:185], v58 offset:26112\n" \
  "ds_read_b64_tr_b16 a[186:187], v58 offset:26624\nds_read_b64_tr_b16 a[188:189], v59 offset:26112\n" \
  "ds_read_b64_tr_b16 a[190:191], v59 offset:26624\ns_mov_b32 %[k], %[hq]\n" \
  "6:\ns_cmp_ge_u32 %[k], 5\n" \
  "s_cbranch_scc1 60f\ns_cmp_ge_u32 %[k], 5\n" \
  "s_cselect_b32 m0, -5, 9\ns_add_u32 m0, m0, %[k]\n" \
  "s_lshl_b32 m0, m0, 17\ns_add_u32 %[k], %[k], %[nh]\n" \
  "buffer_load_dword v60, %[v2], %[rw2], m0 offen\ns_branch 6b\n" \
  "60:\nbuffer_load_dwordx2 v[96:97], %[vx], %[rx], %[sx] offen offset:0\n" \
  "buffer_load_dwordx2 v[98:99], %[vx], %[rx], %[sx] offen offset:256\nbuffer_load_dwordx2 v[100:101], %[vx], %[rx], %[sx] offen offset:512\n" \
  "buffer_load_dwordx2 v[102:103], %[vx], %[rx], %[sx] offen offset:768\nbuffer_load_dwordx2 v[104:105], %[vx], %[rx], %[sx] offen offset:1024\n" \
  "s_movk_i32 %[k], 0x1000\n8:\n" \
  "buffer_load_dword v60, %[vq], %[ry], %[sy] offen sc1\ns_waitcnt vmcnt(0)\n" \
  "v_cmp_eq_u32 vcc, -1, v60\ns_cbranch_vccz 9f\n" \
  "s_sleep 4\ns_sub_u32 %[k], %[k], 1\n" \
  "s_cbranch_scc0 8b\n9:\n" \
  "buffer_load_dwordx2 v[106:107], %[vy], %[ry], %[sy] offen offset:0 sc1\nbuffer_load_dwordx2 v[108:109], %[vy], %[ry], %[sy] offen offset:2048 sc1\n" \
  "s_add_u32 %[t], %[sy], 4096\nbuffer_load_dwordx2 v[110:111], %[vy], %[ry], %[t] offen offset:0 sc1\n" \
  "buffer_load_dwordx2 v[112:113], %[vy], %[ry], %[t] offen offset:2048 sc1\ns_add_u32 %[t], %[sy], 8192\n" \
  "buffer_load_dwordx2 v[114:115], %[vy], %[ry], %[t] offen offset:0 sc1\ns_waitcnt vmcnt(0) lgkmcnt(0)\n" \
  ""
#define G1_ASM \
  "s_movk_i32 %[k], 96\nv_xad_u32 v56, %[xc], 0, %[rb]\n" \
  "v_xad_u32 v57, %[xc], 32, %[rb]\nv_xad_u32 v58, %[xc], 64, %[rb]\n" \
  "v_xad_u32 v59, %[xc], %[k], %[rb]\ns_mov_b32 %[n], 262144\n" \
  "s_branch 3f\n100:\n" \
  "s_sub_u32 %[n], %[n], 1\ns_cbranch_scc1 300f\n" \
  "s_add_u32 %[u], %[sa], 0\nbuffer_load_dwordx4 v[116:119], %[va], %[ra], %[u] offen offset:0 sc1\n" \
  "buffer_load_dwordx4 v[120:123], %[va], %[ra], %[u] offen offset:256 sc1\nbuffer_load_dwordx4 v[124:127], %[va], %[ra], %[u] offen offset:512 sc1\n" \
  "buffer_load_dwordx4 v[128:131], %[va], %[ra], %[u] offen offset:768 sc1\nbuffer_load_dwordx4 v[132:135], %[va], %[ra], %[u] offen offset:1024 sc1\n" \
  "s_waitcnt vmcnt(0)\nv_max_u32 v60, v116, v118\n" \
  "v_max3_u32 v60, v60, v120, v122\nv_max3_u32 v60, v60, v124, v126\n" \
  "v_max3_u32 v60, v60, v128, v130\nv_max3_u32 v60, v60, v132, v134\n" \
  "v_cmp_eq_u32 vcc, -1, v60\ns_cbranch_vccnz 100b\n" \
  "s_branch 200f\n300:\n" \
  "s_mov_b32 %[n], 0\ns_branch 200f\n" \
  "101:\ns_sub_u32 %[n], %[n], 1\n" \
  "s_cbranch_scc1 301f\ns_add_u32 %[u], %[sa], 5120\n" \
  "buffer_load_dwordx4 v[136:139], %[va], %[ra], %[u] offen offset:0 sc1\nbuffer_load_dwordx4 v[140:143], %[va], %[ra], %[u] offen offset:256 sc1\n" \
  "buffer_load_dwordx4 v[144:147], %[va], %[ra], %[u] offen offset:512 sc1\nbuffer_load_dwordx4 v[148:151], %[va], %[ra], %[u] offen offset:768 sc1\n" \
  "buffer_load_dwordx4 v[152:155], %[va], %[ra], %[u] offen offset:1024 sc1\ns_waitcnt vmcnt(0)\n" \
  "v_max_u32 v60, v136, v138\nv_max3_u32 v60, v60, v140, v142\n" \
  "v_max3_u32 v60, v60, v144, v146\nv_max3_u32 v60, v60, v148, v150\n" \
  "v_max3_u32 v60, v60, v152, v154\nv_cmp_eq_u32 vcc, -1, v60\n" \
  "s_cbranch_vccnz 101b\ns_branch 201f\n" \
  "301:\ns_mov_b32 %[n], 0\n" \
  "s_branch 201f\n102:\n" \
  "s_sub_u32 %[n], %[n], 1\ns_cbranch_scc1 302f\n" \
  "s_add_u32 %[u], %[sa], 10240\nbuffer_load_dwordx4 v[156:159], %[va], %[ra], %[u] offen offset:0 sc1\n" \
  "buffer_load_dwordx4 v[160:163], %[va], %[ra], %[u] offen offset:256 sc1\nbuffer_load_dwordx4 v[164:167], %[va], %[ra], %[u] offen offset:512 sc1\n" \
  "buffer_load_dwordx4 v[168:171], %[va], %[ra], %[u] offen offset:768 sc1\nbuffer_load_dwordx4 v[172:175], %[va], %[ra], %[u] offen offset:1024 sc1\n" \
  "s_waitcnt vmcnt(0)\nv_max_u32 v60, v156, v158\n" \
  "v_max3_u32 v60, v60, v160, v162\nv_max3_u32 v60, v60, v164, v166\n" \
  "v_max3_u32 v60, v60, v168, v170\nv_max3_u32 v60, v60, v172, v174\n" \
  "v_cmp_eq_u32 vcc, -1, v60\ns_cbranch_vccnz 102b\n" \
  "s_branch 202f\n302:\n" \
  "s_mov_b32 %[n], 0\ns_branch 202f\n" \
  "103:\ns_sub_u32 %[n], %[n], 1\n" \
  "s_cbranch_scc1 303f\ns_add_u32 %[u], %[sa], 15360\n" \
  "buffer_load_dwordx4 v[176:179], %[va], %[ra], %[u] offen offset:0 sc1\nbuffer_load_dwordx4 v[180:183], %[va], %[ra], %[u] offen offset:256 sc1\n" \
  "buffer_load_dwordx4 v[184:187], %[va], %[ra], %[u] offen offset:512 sc1\nbuffer_load_dwordx4 v[188:191], %[va], %[ra], %[u] offen offset:768 sc1\n" \
  "buffer_load_dwordx4 v[192:195], %[va], %[ra], %[u] offen offset:1024 sc1\ns_waitcnt vmcnt(0)\n" \
  "v_max_u32 v60, v176, v178\nv_max3_u32 v60, v60, v180, v182\n" \
  "v_max3_u32 v60, v60, v184, v186\nv_max3_u32 v60, v60, v188, v190\n" \
  "v_max3_u32 v60, v60, v192, v194\nv_cmp_eq_u32 vcc, -1, v60\n" \
  "s_cbranch_vccnz 103b\ns_branch 203f\n" \
  "303:\ns_mov_b32 %[n], 0\n" \
  "s_branch 203f\n104:\n" \
  "s_sub_u32 %[n], %[n], 1\ns_cbranch_scc1 304f\n" \
  "s_add_u32 %[u], %[sa], 20480\nbuffer_load_dwordx4 v[196:199], %[va], %[ra], %[u] offen offset:0 sc1\n" \
  "buffer_load_dwordx4 v[200:203], %[va], %[ra], %[u] offen offset:256 sc1\nbuffer_load_dwordx4 v[204:207], %[va], %[ra], %[u] offen offset:512 sc1\n" \
  "buffer_load_dwordx4 v[208:211], %[va], %[ra], %[u] offen offset:768 sc1\nbuffer_load_dwordx4 v[212:215], %[va], %[ra], %[u] offen offset:1024 sc1\n" \
  "s_waitcnt vmcnt(0)\nv_max_u32 v60, v196, v198\n" \
  "v_max3_u32 v60, v60, v200, v202\nv_max3_u32 v60, v60, v204, v206\n" \
  "v_max3_u32 v60, v60, v208, v210\nv_max3_u32 v60, v60, v212, v214\n" \
  "v_cmp_eq_u32 vcc, -1, v60\ns_cbranch_vccnz 104b\n" \
  "s_branch 204f\n304:\n" \
  "s_mov_b32 %[n], 0\ns_branch 204f\n" \
  "105:\ns_sub_u32 %[n], %[n], 1\n" \
  "s_cbranch_scc1 305f\ns_add_u32 %[u], %[sa], 25600\n" \
  "buffer_load_dwordx4 v[216:219], %[va], %[ra], %[u] offen offset:0 sc1\nbuffer_load_dwordx4 v[220:223], %[va], %[ra], %[u] offen offset:256 sc1\n" \
  "buffer_load_dwordx4 v[224:227], %[va], %[ra], %[u] offen offset:512 sc1\nbuffer_load_dwordx4 v[228:231], %[va], %[ra], %[u] offen offset:768 sc1\n" \
  "buffer_load_dwordx4 v[232:235], %[va], %[ra], %[u] offen offset:1024 sc1\ns_waitcnt vmcnt(0)\n" \
  "v_max_u32 v60, v216, v218\nv_max3_u32 v60, v60, v220, v222\n" \
  "v_max3_u32 v60, v60, v224, v226\nv_max3_u32 v60, v60, v228, v230\n" \
  "v_max3_u32 v60, v60, v232, v234\nv_cmp_eq_u32 vcc, -1, v60\n" \
  "s_cbranch_vccnz 105b\ns_branch 205f\n" \
  "305:\ns_mov_b32 %[n], 0\n" \
  "s_branch 205f\n106:\n" \
  "s_sub_u32 %[n], %[n], 1\ns_cbranch_scc1 306f\n" \
  "s_add_u32 %[u], %[sa], 30720\nbuffer_load_dwordx4 v[236:239], %[va], %[ra], %[u] offen offset:0 sc1\n" \
  "buffer_load_dwordx4 v[240:243], %[va], %[ra], %[u] offen offset:256 sc1\nbuffer_load_dwordx4 v[244:247], %[va], %[ra], %[u] offen offset:512 sc1\n" \
  "buffer_load_dwordx4 v[248:251], %[va], %[ra], %[u] offen offset:768 sc1\nbuffer_load_dwordx4 v[252:255], %[va], %[ra], %[u] offen offset:1024 sc1\n" \
  "s_waitcnt vmcnt(0)\nv_max_u32 v60, v236, v238\n" \
  "v_max3_u32 v60, v60, v240, v242\nv_max3_u32 v60, v60, v244, v246\n" \
  "v_max3_u32 v60, v60, v248, v250\nv_max3_u32 v60, v60, v252, v254\n" \
  "v_cmp_eq_u32 vcc, -1, v60\ns_cbranch_vccnz 106b\n" \
  "s_branch 206f\n306:\n" \
  "s_mov_b32 %[n], 0\ns_branch 206f\n" \
  "107:\ns_sub_u32 %[n], %[n], 1\n" \
  "s_cbranch_scc1 307f\ns_add_u32 %[u], %[sa], 35840\n" \
  "buffer_load_dwordx4 v[116:119], %[va], %[ra], %[u] offen offset:0 sc1\nbuffer_load_dwordx4 v[120:123], %[va], %[ra], %[u] offen offset:256 sc1\n" \
  "buffer_load_dwordx4 v[124:127], %[va], %[ra], %[u] offen offset:512 sc1\nbuffer_load_dwordx4 v[128:131], %[va], %[ra], %[u] offen offset:768 sc1\n" \
  "buffer_load_dwordx4 v[132:135], %[va], %[ra], %[u] offen offset:1024 sc1\ns_waitcnt vmcnt(0)\n" \
  "v_max_u32 v60, v116, v118\nv_max3_u32 v60, v60, v120, v122\n" \
  "v_max3_u32 v60, v60, v124, v126\nv_max3_u32 v60, v60, v128, v130\n" \
  "v_max3_u32 v60, v60, v132, v134\nv_cmp_eq_u32 vcc, -1, v60\n" \
  "s_cbranch_vccnz 107b\ns_branch 207f\n" \
  "307:\ns_mov_b32 %[n], 0\n" \
  "s_branch 207f\n108:\n" \
  "s_sub_u32 %[n], %[n], 1\ns_cbranch_scc1 308f\n" \
  "s_add_u32 %[u], %[sa], 40960\nbuffer_load_dwordx4 v[136:139], %[va], %[ra], %[u] offen offset:0 sc1\n" \
  "buffer_load_dwordx4 v[140:143], %[va], %[ra], %[u] offen offset:256 sc1\nbuffer_load_dwordx4 v[144:147], %[va], %[ra], %[u] offen offset:512 sc1\n" \
  "buffer_load_dwordx4 v[148:151], %[va], %[ra], %[u] offen offset:768 sc1\nbuffer_load_dwordx4 v[152:155], %[va], %[ra], %[u] offen offset:1024 sc1\n" \
  "s_waitcnt vmcnt(0)\nv_max_u32 v60, v136, v138\n" \
  "v_max3_u32 v60, v60, v140, v142\nv_max3_u32 v60, v60, v144, v146\n" \
  "v_max3_u32 v60, v60, v148, v150\nv_max3_u32 v60, v60, v152, v154\n" \
  "v_cmp_eq_u32 vcc, -1, v60\ns_cbranch_vccnz 108b\n" \
  "s_branch 208f\n308:\n" \
  "s_mov_b32 %[n], 0\ns_branch 208f\n" \
  "109:\ns_sub_u32 %[n], %[n], 1\n" \
  "s_cbranch_scc1 309f\ns_add_u32 %[u], %[sa], 46080\n" \
  "buffer_load_dwordx4 v[156:159], %[va], %[ra], %[u] offen offset:0 sc1\nbuffer_load_dwordx4 v[160:163], %[va], %[ra], %[u] offen offset:256 sc1\n" \
  "buffer_load_dwordx4 v[164:167], %[va], %[ra], %[u] offen offset:512 sc1\nbuffer_load_dwordx4 v[168:171], %[va], %[ra], %[u] offen offset:768 sc1\n" \
  "buffer_load_dwordx4 v[172:175], %[va], %[ra], %[u] offen offset:1024 sc1\ns_waitcnt vmcnt(0)\n" \
  "v_max_u32 v60, v156, v158\nv_max3_u32 v60, v60, v160, v162\n" \
  "v_max3_u32 v60, v60, v164, v166\nv_max3_u32 v60, v60, v168, v170\n" \
  "v_max3_u32 v60, v60, v172, v174\nv_cmp_eq_u32 vcc, -1, v60\n" \
  "s_cbranch_vccnz 109b\ns_branch 209f\n" \
  "309:\ns_mov_b32 %[n], 0\n" \
  "s_branch 209f\n110:\n" \
  "s_sub_u32 %[n], %[n], 1\ns_cbranch_scc1 310f\n" \
  "s_add_u32 %[u], %[sa], 51200\nbuffer_load_dwordx4 v[176:179], %[va], %[ra], %[u] offen offset:0 sc1\n" \
  "buffer_load_dwordx4 v[180:183], %[va], %[ra], %[u] offen offset:256 sc1\nbuffer_load_dwordx4 v[184:187], %[va], %[ra], %[u] offen offset:512 sc1\n" \
  "buffer_load_dwordx4 v[188:191], %[va], %[ra], %[u] offen offset:768 sc1\nbuffer_load_dwordx4 v[192:195], %[va], %[ra], %[u] offen offset:1024 sc1\n" \
  "s_waitcnt vmcnt(0)\nv_max_u32 v60, v176, v178\n" \
  "v_max3_u32 v60, v60, v180, v182\nv_max3_u32 v60, v60, v184, v186\n" \
  "v_max3_u32 v60, v60, v188, v190\nv_max3_u32 v60, v60, v192, v194\n" \
  "v_cmp_eq_u32 vcc, -1, v60\ns_cbranch_vccnz 110b\n" \
  "s_branch 210f\n310:\n" \
  "s_mov_b32 %[n], 0\ns_branch 210f\n" \
  "111:\ns_sub_u32 %[n], %[n], 1\n" \
  "s_cbranch_scc1 311f\ns_add_u32 %[u], %[sa], 56320\n" \
  "buffer_load_dwordx4 v[196:199], %[va], %[ra], %[u] offen offset:0 sc1\nbuffer_load_dwordx4 v[200:203], %[va], %[ra], %[u] offen offset:256 sc1\n" \
  "buffer_load_dwordx4 v[204:207], %[va], %[ra], %[u] offen offset:512 sc1\nbuffer_load_dwordx4 v[208:211], %[va], %[ra], %[u] offen offset:768 sc1\n" \
  "buffer_load_dwordx4 v[212:215], %[va], %[ra], %[u] offen offset:1024 sc1\ns_waitcnt vmcnt(0)\n" \
  "v_max_u32 v60, v196, v198\nv_max3_u32 v60, v60, v200, v202\n" \
  "v_max3_u32 v60, v60, v204, v206\nv_max3_u32 v60, v60, v208, v210\n" \
  "v_max3_u32 v60, v60, v212, v214\nv_cmp_eq_u32 vcc, -1, v60\n" \
  "s_cbranch_vccnz 111b\ns_branch 211f\n" \
  "311:\ns_mov_b32 %[n], 0\n" \
  "s_branch 211f\n112:\n" \
  "s_sub_u32 %[n], %[n], 1\ns_cbranch_scc1 312f\n" \
  "s_add_u32 %[u], %[sa], 61440\nbuffer_load_dwordx4 v[216:219], %[va], %[ra], %[u] offen offset:0 sc1\n" \
  "buffer_load_dwordx4 v[220:223], %[va], %[ra], %[u] offen offset:256 sc1\nbuffer_load_dwordx4 v[224:227], %[va], %[ra], %[u] offen offset:512 sc1\n" \
  "buffer_load_dwordx4 v[228:231], %[va], %[ra], %[u] offen offset:768 sc1\nbuffer_load_dwordx4 v[232:235], %[va], %[ra], %[u] offen offset:1024 sc1\n" \
  "s_waitcnt vmcnt(0)\nv_max_u32 v60, v216, v218\n" \
  "v_max3_u32 v60, v60, v220, v222\nv_max3_u32 v60, v60, v224, v226\n" \
  "v_max3_u32 v60, v60, v228, v230\nv_max3_u32 v60, v60, v232, v234\n" \
  "v_cmp_eq_u32 vcc, -1, v60\ns_cbranch_vccnz 112b\n" \
  "s_branch 212f\n312:\n" \
  "s_mov_b32 %[n], 0\ns_branch 212f\n" \
  "113:\ns_sub_u32 %[n], %[n], 1\n" \
  "s_cbranch_scc1 313f\ns_add_u32 %[u], %[sa], 66560\n" \
  "buffer_load_dwordx4 v[236:239], %[va], %[ra], %[u] offen offset:0 sc1\nbuffer_load_dwordx4 v[240:243], %[va], %[ra], %[u] offen offset:256 sc1\n" \
  "buffer_load_dwordx4 v[244:247], %[va], %[ra], %[u] offen offset:512 sc1\nbuffer_load_dwordx4 v[248:251], %[va], %[ra], %[u] offen offset:768 sc1\n" \
  "buffer_load_dwordx4 v[252:255], %[va], %[ra], %[u] offen offset:1024 sc1\ns_waitcnt vmcnt(0)\n" \
  "v_max_u32 v60, v236, v238\nv_max3_u32 v60, v60, v240, v242\n" \
  "v_max3_u32 v60, v60, v244, v246\nv_max3_u32 v60, v60, v248, v250\n" \
  "v_max3_u32 v60, v60, v252, v254\nv_cmp_eq_u32 vcc, -1, v60\n" \
  "s_cbranch_vccnz 113b\ns_branch 213f\n" \
  "313:\ns_mov_b32 %[n], 0\n" \
  "s_branch 213f\n114:\n" \
  "s_sub_u32 %[n], %[n], 1\ns_cbranch_scc1 314f\n" \
  "s_add_u32 %[u], %[sa], 71680\nbuffer_load_dwordx4 v[116:119], %[va], %[ra], %[u] offen offset:0 sc1\n" \
  "buffer_load_dwordx4 v[120:123], %[va], %[ra], %[u] offen offset:256 sc1\nbuffer_load_dwordx4 v[124:127], %[va], %[ra], %[u] offen offset:512 sc1\n" \
  "buffer_load_dwordx4 v[128:131], %[va], %[ra], %[u] offen offset:768 sc1\nbuffer_load_dwordx4 v[132:135], %[va], %[ra], %[u] offen offset:1024 sc1\n" \
  "s_waitcnt vmcnt(0)\nv_max_u32 v60, v116, v118\n" \
  "v_max3_u32 v60, v60, v120, v122\nv_max3_u32 v60, v60, v124, v126\n" \
  "v_max3_u32 v60, v60, v128, v130\nv_max3_u32 v60, v60, v132, v134\n" \
  "v_cmp_eq_u32 vcc, -1, v60\ns_cbranch_vccnz 114b\n" \
  "s_branch 214f\n314:\n" \
  "s_mov_b32 %[n], 0\ns_branch 214f\n" \
  "115:\ns_sub_u32 %[n], %[n], 1\n" \
  "s_cbranch_scc1 315f\ns_add_u32 %[u], %[sa], 76800\n" \
  "buffer_load_dwordx4 v[136:139], %[va], %[ra], %[u] offen offset:0 sc1\nbuffer_load_dwordx4 v[140:143], %[va], %[ra], %[u] offen offset:256 sc1\n" \
  "buffer_load_dwordx4 v[144:147], %[va], %[ra], %[u] offen offset:512 sc1\nbuffer_load_dwordx4 v[148:151], %[va], %[ra], %[u] offen offset:768 sc1\n" \
  "buffer_load_dwordx4 v[152:155], %[va], %[ra], %[u] offen offset:1024 sc1\ns_waitcnt vmcnt(0)\n" \
  "v_max_u32 v60, v136, v138\nv_max3_u32 v60, v60, v140, v142\n" \
  "v_max3_u32 v60, v60, v144, v146\nv_max3_u32 v60, v60, v148, v150\n" \
  "v_max3_u32 v60, v60, v152, v154\nv_cmp_eq_u32 vcc, -1, v60\n" \
  "s_cbranch_vccnz 115b\ns_branch 215f\n" \
  "315:\ns_mov_b32 %[n], 0\n" \
  "s_branch 215f\n3:\n" \
  "buffer_load_dword v61, %[vp], %[ra], %[sa] offen offset:1276 sc1\ns_waitcnt vmcnt(0)\n" \
  "v_cmp_eq_u32 vcc, -1, v61\ns_cbranch_vccz 4f\n" \
  "s_sub_u32 %[n], %[n], 1\ns_cbranch_scc0 3b\n" \
  "s_mov_b32 %[n], 0\n4:\n" \
  "s_mov_b32 %[k], %[sa]\nds_read_b64_tr_b16 v[84:85], v56 offset:30464\n" \
  "ds_read_b64_tr_b16 v[86:87], v56 offset:30976\nds_read_b64_tr_b16 v[88:89], v57 offset:30464\n" \
  "ds_read_b64_tr_b16 v[90:91], v57 offset:30976\nds_read_b64_tr_b16 v[92:93], v58 offset:30464\n" \
  "ds_read_b64_tr_b16 v[94:95], v58 offset:30976\nds_read_b64_tr_b16 v[96:97], v59 offset:30464\n" \
  "ds_read_b64_tr_b16 v[98:99], v59 offset:30976\nbuffer_load_dwordx4 v[116:119], %[va], %[ra], %[k] offen offset:0 sc1\n" \
  "buffer_load_dwordx4 v[120:123], %[va], %[ra], %[k] offen offset:256 sc1\nbuffer_load_dwordx4 v[124:127], %[va], %[ra], %[k] offen offset:512 sc1\n" \
  "buffer_load_dwordx4 v[128:131], %[va], %[ra], %[k] offen offset:768 sc1\nbuffer_load_dwordx4 v[132:135], %[va], %[ra], %[k] offen offset:1024 sc1\n" \
  "s_add_u32 %[k], %[k], 5120\nbuffer_load_dwordx4 v[136:139], %[va], %[ra], %[k] offen offset:0 sc1\n" \
  "buffer_load_dwordx4 v[140:143], %[va], %[ra], %[k] offen offset:256 sc1\nbuffer_load_dwordx4 v[144:147], %[va], %[ra], %[k] offen offset:512 sc1\n" \
  "buffer_load_dwordx4 v[148:151], %[va], %[ra], %[k] offen offset:768 sc1\nbuffer_load_dwordx4 v[152:155], %[va], %[ra], %[k] offen offset:1024 sc1\n" \
  "s_add_u32 %[k], %[k], 5120\nbuffer_load_dwordx4 v[156:159], %[va], %[ra], %[k] offen offset:0 sc1\n" \
  "buffer_load_dwordx4 v[160:163], %[va], %[ra], %[k] offen offset:256 sc1\nbuffer_load_dwordx4 v[164:167], %[va], %[ra], %[k] offen offset:512 sc1\n" \
  "buffer_load_dwordx4 v[168:171], %[va], %[ra], %[k] offen offset:768 sc1\nbuffer_load_dwordx4 v[172:175], %[va], %[ra], %[k] offen offset:1024 sc1\n" \
  "s_add_u32 %[k], %[k], 5120\nbuffer_load_dwordx4 v[176:179], %[va], %[ra], %[k] offen offset:0 sc1\n" \
  "buffer_load_dwordx4 v[180:183], %[va], %[ra], %[k] offen offset:256 sc1\nbuffer_load_dwordx4 v[184:187], %[va], %[ra], %[k] offen offset:512 sc1\n" \
  "buffer_load_dwordx4 v[188:191], %[va], %[ra], %[k] offen offset:768 sc1\nbuffer_load_dwordx4 v[192:195], %[va], %[ra], %[k] offen offset:1024 sc1\n" \
  "s_add_u32 %[k], %[k], 5120\ns_xor_b32 %[u], %[wv], 1\n" \
  "s_mul_i32 %[u], %[u], 40960\nv_add_u32 v61, %[u], %[vl]\n" \
  "s_xor_b32 %[u], %[wv], 2\ns_mul_i32 %[u], %[u], 40960\n" \
  "v_add_u32 v62, %[u], %[vl]\ns_xor_b32 %[u], %[wv], 3\n" \
  "s_mul_i32 %[u], %[u], 40960\nv_add_u32 v63, %[u], %[vl]\n" \
  "s_waitcnt vmcnt(15)\nv_max_u32 v60, v116, v118\n" \
  "v_max3_u32 v60, v60, v120, v122\nv_max3_u32 v60, v60, v124, v126\n" \
  "v_max3_u32 v60, v60, v128, v130\nv_max3_u32 v60, v60, v132, v134\n" \
  "v_cmp_eq_u32 vcc, -1, v60\ns_cbranch_vccnz 100b\n" \
  "200:\ns_waitcnt lgkmcnt(0)\n" \
  "v_mfma_f32_16x16x32_bf16 v[64:67], v[84:87], v[116:119], 0\nbuffer_load_dwordx4 v[196:199], %[va], %[ra], %[k] offen offset:0 sc1\n" \
  "v_mfma_f32_16x16x32_bf16 v[68:71], v[84:87], v[120:123], 0\nds_read_b64_tr_b16 v[100:101], v56 offset:0\n" \
  "v_mfma_f32_16x16x32_bf16 v[72:75], v[84:87], v[124:127], 0\nds_read_b64_tr_b16 v[102:103], v56 offset:512\n" \
  "v_mfma_f32_16x16x32_bf16 v[76:79], v[84:87], v[128:131], 0\nds_read_b64_tr_b16 v[104:105], v57 offset:0\n" \
  "v_mfma_f32_16x16x32_bf16 v[80:83], v[84:87], v[132:135], 0\nbuffer_load_dwordx4 v[200:203], %[va], %[ra], %[k] offen offset:256 sc1\n" \
  "v_mfma_f32_16x16x32_bf16 a[192:195], v[88:91], v[116:119], 0\nds_read_b64_tr_b16 v[106:107], v57 offset:512\n" \
  "v_mfma_f32_16x16x32_bf16 a[196:199], v[88:91], v[120:123], 0\nds_read_b64_tr_b16 v[108:109], v58 offset:0\n" \
  "v_mfma_f32_16x16x32_bf16 a[200:203], v[88:91], v[124:127], 0\nds_read_b64_tr_b16 v[110:111], v58 offset:512\n" \
  "v_mfma_f32_16x16x32_bf16 a[204:207], v[88:91], v[128:131], 0\nbuffer_load_dwordx4 v[204:207], %[va], %[ra], %[k] offen offset:512 sc1\n" \
  "v_mfma_f32_16x16x32_bf16 a[208:211], v[88:91], v[132:135], 0\nds_read_b64_tr_b16 v[112:113], v59 offset:0\n" \
  "v_mfma_f32_16x16x32_bf16 a[212:215], v[92:95], v[116:119], 0\nds_read_b64_tr_b16 v[114:115], v59 offset:512\n" \
  "v_mfma_f32_16x16x32_bf16 a[216:219], v[92:95], v[120:123], 0\ns_waitcnt vmcnt(13)\n" \
  "v_max_u32 v60, v136, v138\nv_mfma_f32_16x16x32_bf16 a[220:223], v[92:95], v[124:127], 0\n" \
  "buffer_load_dwordx4 v[208:211], %[va], %[ra], %[k] offen offset:768 sc1\nv_mfma_f32_16x16x32_bf16 a[224:227], v[92:95], v[128:131], 0\n" \
  "v_max3_u32 v60, v60, v140, v142\nv_mfma_f32_16x16x32_bf16 a[228:231], v[92:95], v[132:135], 0\n" \
  "v_max3_u32 v60, v60, v144, v146\nv_mfma_f32_16x16x32_bf16 a[232:235], v[96:99], v[116:119], 0\n" \
  "v_max3_u32 v60, v60, v148, v150\nv_mfma_f32_16x16x32_bf16 a[236:239], v[96:99], v[120:123], 0\n" \
  "buffer_load_dwordx4 v[212:215], %[va], %[ra], %[k] offen offset:1024 sc1\ns_add_u32 %[k], %[k], 5120\n" \
  "v_mfma_f32_16x16x32_bf16 a[240:243], v[96:99], v[124:127], 0\nv_max3_u32 v60, v60, v152, v154\n" \
  "v_mfma_f32_16x16x32_bf16 a[244:247], v[96:99], v[128:131], 0\nv_cmp_eq_u32 vcc, -1, v60\n" \
  "v_mfma_f32_16x16x32_bf16 a[248:251], v[96:99], v[132:135], 0\ns_cbranch_vccnz 101b\n" \
  "201:\ns_waitcnt lgkmcnt(0)\n" \
  "v_mfma_f32_16x16x32_bf16 v[64:67], v[100:103], v[136:139], v[64:67]\nbuffer_load_dwordx4 v[216:219], %[va], %[ra], %[k] offen offset:0 sc1\n" \
  "v_mfma_f32_16x16x32_bf16 v[68:71], v[100:103], v[140:143], v[68:71]\nds_read_b64_tr_b16 v[84:85], v56 offset:4352\n" \
  "v_mfma_f32_16x16x32_bf16 v[72:75], v[100:103], v[144:147], v[72:75]\nds_read_b64_tr_b16 v[86:87], v56 offset:4864\n" \
  "v_mfma_f32_16x16x32_bf16 v[76:79], v[100:103], v[148:151], v[76:79]\nds_read_b64_tr_b16 v[88:89], v57 offset:4352\n" \
  "v_mfma_f32_16x16x32_bf16 v[80:83], v[100:103], v[152:155], v[80:83]\nbuffer_load_dwordx4 v[220:223], %[va], %[ra], %[k] offen offset:256 sc1\n" \
  "v_mfma_f32_16x16x32_bf16 a[192:195], v[104:107], v[136:139], a[192:195]\nds_read_b64_tr_b16 v[90:91], v57 offset:4864\n" \
  "v_mfma_f32_16x16x32_bf16 a[196:199], v[104:107], v[140:143], a[196:199]\nds_read_b64_tr_b16 v[92:93], v58 offset:4352\n" \
  "v_mfma_f32_16x16x32_bf16 a[200:203], v[104:107], v[144:147], a[200:203]\nds_read_b64_tr_b16 v[94:95], v58 offset:4864\n" \
  "v_mfma_f32_16x16x32_bf16 a[204:207], v[104:107], v[148:151], a[204:207]\nbuffer_load_dwordx4 v[224:227], %[va], %[ra], %[k] offen offset:512 sc1\n" \
  "v_mfma_f32_16x16x32_bf16 a[208:211], v[104:107], v[152:155], a[208:211]\nds_read_b64_tr_b16 v[96:97], v59 offset:4352\n" \
  "v_mfma_f32_16x16x32_bf16 a[212:215], v[108:111], v[136:139], a[212:215]\nds_read_b64_tr_b16 v[98:99], v59 offset:4864\n" \
  "v_mfma_f32_16x16x32_bf16 a[216:219], v[108:111], v[140:143], a[216:219]\ns_waitcnt vmcnt(13)\n" \
  "v_max_u32 v60, v156, v158\nv_mfma_f32_16x16x32_bf16 a[220:223], v[108:111], v[144:147], a[220:223]\n" \
  "buffer_load_dwordx4 v[228:231], %[va], %[ra], %[k] offen offset:768 sc1\nv_mfma_f32_16x16x32_bf16 a[224:227], v[108:111], v[148:151], a[224:227]\n" \
  "v_max3_u32 v60, v60, v160, v162\nv_mfma_f32_16x16x32_bf16 a[228:231], v[108:111], v[152:155], a[228:231]\n" \
  "v_max3_u32 v60, v60, v164, v166\nv_mfma_f32_16x16x32_bf16 a[232:235], v[112:115], v[136:139], a[232:235]\n" \
  "v_max3_u32 v60, v60, v168, v170\nv_mfma_f32_16x16x32_bf16 a[236:239], v[112:115], v[140:143], a[236:239]\n" \
  "buffer_load_dwordx4 v[232:235], %[va], %[ra], %[k] offen offset:1024 sc1\ns_add_u32 %[k], %[k], 5120\n" \
  "v_mfma_f32_16x16x32_bf16 a[240:243], v[112:115], v[144:147], a[240:243]\nv_max3_u32 v60, v60, v172, v174\n" \
  "v_mfma_f32_16x16x32_bf16 a[244:247], v[112:115], v[148:151], a[244:247]\nv_cmp_eq_u32 vcc, -1, v60\n" \
  "v_mfma_f32_16x16x32_bf16 a[248:251], v[112:115], v[152:155], a[248:251]\ns_cbranch_vccnz 102b\n" \
  "202:\ns_waitcnt lgkmcnt(0)\n" \
  "v_mfma_f32_16x16x32_bf16 v[64:67], v[84:87], v[156:159], v[64:67]\nbuffer_load_dwordx4 v[236:239], %[va], %[ra], %[k] offen offset:0 sc1\n" \
  "v_mfma_f32_16x16x32_bf16 v[68:71], v[84:87], v[160:163], v[68:71]\nds_read_b64_tr_b16 v[100:101], v56 offset:8704\n" \
  "v_mfma_f32_16x16x32_bf16 v[72:75], v[84:87], v[164:167], v[72:75]\nds_read_b64_tr_b16 v[102:103], v56 offset:9216\n" \
  "v_mfma_f32_16x16x32_bf16 v[76:79], v[84:87], v[168:171], v[76:79]\nbuffer_load_dwordx4 v[240:243], %[va], %[ra], %[k] offen offset:256 sc1\n" \
  "v_mfma_f32_16x16x32_bf16 v[80:83], v[84:87], v[172:175], v[80:83]\nds_read_b64_tr_b16 v[104:105], v57 offset:8704\n" \
  "v_mfma_f32_16x16x32_bf16 a[192:195], v[88:91], v[156:159], a[192:195]\nds_read_b64_tr_b16 v[106:107], v57 offset:9216\n" \
  "v_mfma_f32_16x16x32_bf16 a[196:199], v[88:91], v[160:163], a[196:199]\ns_add_u32 m0, %[l], 0\n" \
  "s_add_u32 %[t], %[sw2], 1703936\nbuffer_load_dwordx4 %[vw2], %[rw2], %[t] offen lds\n" \
  "v_mfma_f32_16x16x32_bf16 a[200:203], v[88:91], v[164:167], a[200:203]\nds_read_b64_tr_b16 v[108:109], v58 offset:8704\n" \
  "v_mfma_f32_16x16x32_bf16 a[204:207], v[88:91], v[168:171], a[204:207]\nds_read_b64_tr_b16 v[110:111], v58 offset:9216\n" \
  "v_mfma_f32_16x16x32_bf16 a[208:211], v[88:91], v[172:175], a[208:211]\nds_read_b64_tr_b16 v[112:113], v59 offset:8704\n" \
  "v_mfma_f32_16x16x32_bf16 a[212:215], v[92:95], v[156:159], a[212:215]\nbuffer_load_dwordx4 v[244:247], %[va], %[ra], %[k] offen offset:512 sc1\n" \
  "v_mfma_f32_16x16x32_bf16 a[216:219], v[92:95], v[160:163], a[216:219]\nds_read_b64_tr_b16 v[114:115], v59 offset:9216\n" \
  "v_mfma_f32_16x16x32_bf16 a[220:223], v[92:95], v[164:167], a[220:223]\ns_waitcnt vmcnt(14)\n" \
  "v_max_u32 v60, v176, v178\nv_mfma_f32_16x16x32_bf16 a[224:227], v[92:95], v[168:171], a[224:227]\n" \
  "buffer_load_dwordx4 v[248:251], %[va], %[ra], %[k] offen offset:768 sc1\nv_mfma_f32_16x16x32_bf16 a[228:231], v[92:95], v[172:175], a[228:231]\n" \
  "v_max3_u32 v60, v60, v180, v182\nv_mfma_f32_16x16x32_bf16 a[232:235], v[96:99], v[156:159], a[232:235]\n" \
  "v_max3_u32 v60, v60, v184, v186\nv_mfma_f32_16x16x32_bf16 a[236:239], v[96:99], v[160:163], a[236:239]\n" \
  "buffer_load_dwordx4 v[252:255], %[va], %[ra], %[k] offen offset:1024 sc1\ns_add_u32 %[k], %[k], 5120\n" \
  "v_mfma_f32_16x16x32_bf16 a[240:243], v[96:99], v[164:167], a[240:243]\nv_max3_u32 v60, v60, v188, v190\n" \
  "v_mfma_f32_16x16x32_bf16 a[244:247], v[96:99], v[168:171], a[244:247]\nv_max3_u32 v60, v60, v192, v194\n" \
  "v_mfma_f32_16x16x32_bf16 a[248:251], v[96:99], v[172:175], a[248:251]\nv_cmp_eq_u32 vcc, -1, v60\n" \
  "s_cbranch_vccnz 103b\n203:\n" \
  "s_waitcnt lgkmcnt(0)\nv_mfma_f32_16x16x32_bf16 v[64:67], v[100:103], v[176:179], v[64:67]\n" \
  "buffer_load_dwordx4 v[116:119], %[va], %[ra], %[k] offen offset:0 sc1\nv_mfma_f32_16x16x32_bf16 v[68:71], v[100:103], v[180:183], v[68:71]\n" \
  "v_mfma_f32_16x16x32_bf16 v[72:75], v[100:103], v[184:187], v[72:75]\nv_mfma_f32_16x16x32_bf16 v[76:79], v[100:103], v[188:191], v[76:79]\n" \
  "buffer_load_dwordx4 v[120:123], %[va], %[ra], %[k] offen offset:256 sc1\nv_mfma_f32_16x16x32_bf16 v[80:83], v[100:103], v[192:195], v[80:83]\n" \
  "v_mfma_f32_16x16x32_bf16 a[192:195], v[104:107], v[176:179], a[192:195]\nv_mfma_f32_16x16x32_bf16 a[196:199], v[104:107], v[180:183], a[196:199]\n" \
  "s_add_u32 m0, %[l], 1088\ns_add_u32 %[t], %[sw2], 1736704\n" \
  "buffer_load_dwordx4 %[vw2], %[rw2], %[t] offen lds\nv_mfma_f32_16x16x32_bf16 a[200:203], v[104:107], v[184:187], a[200:203]\n" \
  "v_mfma_f32_16x16x32_bf16 a[204:207], v[104:107], v[188:191], a[204:207]\nv_mfma_f32_16x16x32_bf16 a[208:211], v[104:107], v[192:195], a[208:211]\n" \
  "v_mfma_f32_16x16x32_bf16 a[212:215], v[108:111], v[176:179], a[212:215]\nbuffer_load_dwordx4 v[124:127], %[va], %[ra], %[k] offen offset:512 sc1\n" \
  "v_mfma_f32_16x16x32_bf16 a[216:219], v[108:111], v[180:183], a[216:219]\ns_waitcnt vmcnt(15)\n" \
  "v_max_u32 v60, v196, v198\nv_mfma_f32_16x16x32_bf16 a[220:223], v[108:111], v[184:187], a[220:223]\n" \
  "v_max3_u32 v60, v60, v200, v202\nv_mfma_f32_16x16x32_bf16 a[224:227], v[108:111], v[188:191], a[224:227]\n" \
  "buffer_load_dwordx4 v[128:131], %[va], %[ra], %[k] offen offset:768 sc1\nv_mfma_f32_16x16x32_bf16 a[228:231], v[108:111], v[192:195], a[228:231]\n" \
  "v_max3_u32 v60, v60, v204, v206\nv_mfma_f32_16x16x32_bf16 a[232:235], v[112:115], v[176:179], a[232:235]\n" \
  "v_max3_u32 v60, v60, v208, v210\nv_mfma_f32_16x16x32_bf16 a[236:239], v[112:115], v[180:183], a[236:239]\n" \
  "buffer_load_dwordx4 v[132:135], %[va], %[ra], %[k] offen offset:1024 sc1\ns_add_u32 %[k], %[k], 5120\n" \
  "v_mfma_f32_16x16x32_bf16 a[240:243], v[112:115], v[184:187], a[240:243]\nv_max3_u32 v60, v60, v212, v214\n" \
  "v_mfma_f32_16x16x32_bf16 a[244:247], v[112:115], v[188:191], a[244:247]\nv_cmp_eq_u32 vcc, -1, v60\n" \
  "v_mfma_f32_16x16x32_bf16 a[248:251], v[112:115], v[192:195], a[248:251]\ns_cbranch_vccnz 104b\n" \
  "204:\nv_mfma_f32_16x16x32_bf16 v[64:67], a[0:3], v[196:199], v[64:67]\n" \
  "buffer_load_dwordx4 v[136:139], %[va], %[ra], %[k] offen offset:0 sc1\nv_mfma_f32_16x16x32_bf16 v[68:71], a[0:3], v[200:203], v[68:71]\n" \
  "v_mfma_f32_16x16x32_bf16 v[72:75], a[0:3], v[204:207], v[72:75]\nv_mfma_f32_16x16x32_bf16 v[76:79], a[0:3], v[208:211], v[76:79]\n" \
  "buffer_load_dwordx4 v[140:143], %[va], %[ra], %[k] offen offset:256 sc1\nv_mfma_f32_16x16x32_bf16 v[80:83], a[0:3], v[212:215], v[80:83]\n" \
  "v_mfma_f32_16x16x32_bf16 a[192:195], a[4:7], v[196:199], a[192:195]\nv_mfma_f32_16x16x32_bf16 a[196:199], a[4:7], v[200:203], a[196:199]\n" \
  "s_add_u32 m0, %[l], 2176\ns_add_u32 %[t], %[sw2], 1769472\n" \
  "buffer_load_dwordx4 %[vw2], %[rw2], %[t] offen lds\nv_mfma_f32_16x16x32_bf16 a[200:203], a[4:7], v[204:207], a[200:203]\n" \
  "v_mfma_f32_16x16x32_bf16 a[204:207], a[4:7], v[208:211], a[204:207]\nv_mfma_f32_16x16x32_bf16 a[208:211], a[4:7], v[212:215], a[208:211]\n" \
  "v_mfma_f32_16x16x32_bf16 a[212:215], a[8:11], v[196:199], a[212:215]\nbuffer_load_dwordx4 v[144:147], %[va], %[ra], %[k] offen offset:512 sc1\n" \
  "v_mfma_f32_16x16x32_bf16 a[216:219], a[8:11], v[200:203], a[216:219]\ns_waitcnt vmcnt(16)\n" \
  "v_max_u32 v60, v216, v218\nv_mfma_f32_16x16x32_bf16 a[220:223], a[8:11], v[204:207], a[220:223]\n" \
  "v_max3_u32 v60, v60, v220, v222\nv_mfma_f32_16x16x32_bf16 a[224:227], a[8:11], v[208:211], a[224:227]\n" \
  "buffer_load_dwordx4 v[148:151], %[va], %[ra], %[k] offen offset:768 sc1\nv_mfma_f32_16x16x32_bf16 a[228:231], a[8:11], v[212:215], a[228:231]\n" \
  "v_max3_u32 v60, v60, v224, v226\nv_mfma_f32_16x16x32_bf16 a[232:235], a[12:15], v[196:199], a[232:235]\n" \
  "v_max3_u32 v60, v60, v228, v230\nv_mfma_f32_16x16x32_bf16 a[236:239], a[12:15], v[200:203], a[236:239]\n" \
  "buffer_load_dwordx4 v[152:155], %[va], %[ra], %[k] offen offset:1024 sc1\ns_add_u32 %[k], %[k], 5120\n" \
  "v_mfma_f32_16x16x32_bf16 a[240:243], a[12:15], v[204:207], a[240:243]\nv_max3_u32 v60, v60, v232, v234\n" \
  "v_mfma_f32_16x16x32_bf16 a[244:247], a[12:15], v[208:211], a[244:247]\nv_cmp_eq_u32 vcc, -1, v60\n" \
  "v_mfma_f32_16x16x32_bf16 a[248:251], a[12:15], v[212:215], a[248:251]\ns_cbranch_vccnz 105b\n" \
  "205:\nv_mfma_f32_16x16x32_bf16 v[64:67], a[16:19], v[216:219], v[64:67]\n" \
  "buffer_load_dwordx4 v[156:159], %[va], %[ra], %[k] offen offset:0 sc1\nv_mfma_f32_16x16x32_bf16 v[68:71], a[16:19], v[220:223], v[68:71]\n" \
  "ds_read_b64_tr_b16 a[0:1], v56 offset:13056\nv_mfma_f32_16x16x32_bf16 v[72:75], a[16:19], v[224:227], v[72:75]\n" \
  "ds_read_b64_tr_b16 a[2:3], v56 offset:13568\nv_mfma_f32_16x16x32_bf16 v[76:79], a[16:19], v[228:231], v[76:79]\n" \
  "buffer_load_dwordx4 v[160:163], %[va], %[ra], %[k] offen offset:256 sc1\nv_mfma_f32_16x16x32_bf16 v[80:83], a[16:19], v[232:235], v[80:83]\n" \
  "ds_read_b64_tr_b16 a[4:5], v57 offset:13056\nv_mfma_f32_16x16x32_bf16 a[192:195], a[20:23], v[216:219], a[192:195]\n" \
  "ds_read_b64_tr_b16 a[6:7], v57 offset:13568\nv_mfma_f32_16x16x32_bf16 a[196:199], a[20:23], v[220:223], a[196:199]\n" \
  "s_add_u32 m0, %[l], 3264\ns_add_u32 %[t], %[sw2], 1802240\n" \
  "buffer_load_dwordx4 %[vw2], %[rw2], %[t] offen lds\nv_mfma_f32_16x16x32_bf16 a[200:203], a[20:23], v[224:227], a[200:203]\n" \
  "ds_read_b64_tr_b16 a[8:9], v58 offset:13056\nv_mfma_f32_16x16x32_bf16 a[204:207], a[20:23], v[228:231], a[204:207]\n" \
  "ds_read_b64_tr_b16 a[10:11], v58 offset:13568\nv_mfma_f32_16x16x32_bf16 a[208:211], a[20:23], v[232:235], a[208:211]\n" \
  "ds_read_b64_tr_b16 a[12:13], v59 offset:13056\nv_mfma_f32_16x16x32_bf16 a[212:215], a[24:27], v[216:219], a[212:215]\n" \
  "buffer_load_dwordx4 v[164:167], %[va], %[ra], %[k] offen offset:512 sc1\nv_mfma_f32_16x16x32_bf16 a[216:219], a[24:27], v[220:223], a[216:219]\n" \
  "ds_read_b64_tr_b16 a[14:15], v59 offset:13568\nv_mfma_f32_16x16x32_bf16 a[220:223], a[24:27], v[224:227], a[220:223]\n" \
  "s_waitcnt vmcnt(16)\nv_max_u32 v60, v236, v238\n" \
  "v_mfma_f32_16x16x32_bf16 a[224:227], a[24:27], v[228:231], a[224:227]\nbuffer_load_dwordx4 v[168:171], %[va], %[ra], %[k] offen offset:768 sc1\n" \
  "v_mfma_f32_16x16x32_bf16 a[228:231], a[24:27], v[232:235], a[228:231]\nv_max3_u32 v60, v60, v240, v242\n" \
  "v_mfma_f32_16x16x32_bf16 a[232:235], a[28:31], v[216:219], a[232:235]\nv_max3_u32 v60, v60, v244, v246\n" \
  "v_mfma_f32_16x16x32_bf16 a[236:239], a[28:31], v[220:223], a[236:239]\nbuffer_load_dwordx4 v[172:175], %[va], %[ra], %[k] offen offset:1024 sc1\n" \
  "s_add_u32 %[k], %[k], 5120\nv_mfma_f32_16x16x32_bf16 a[240:243], a[28:31], v[224:227], a[240:243]\n" \
  "v_max3_u32 v60, v60, v248, v250\nv_mfma_f32_16x16x32_bf16 a[244:247], a[28:31], v[228:231], a[244:247]\n" \
  "v_max3_u32 v60, v60, v252, v254\nv_mfma_f32_16x16x32_bf16 a[248:251], a[28:31], v[232:235], a[248:251]\n" \
  "v_cmp_eq_u32 vcc, -1, v60\ns_cbranch_vccnz 106b\n" \
  "206:\nv_mfma_f32_16x16x32_bf16 v[64:67], a[32:35], v[236:239], v[64:67]\n" \
  "buffer_load_dwordx4 v[176:179], %[va], %[ra], %[k] offen offset:0 sc1\nv_mfma_f32_16x16x32_bf16 v[68:71], a[32:35], v[240:243], v[68:71]\n" \
  "ds_read_b64_tr_b16 a[16:17], v56 offset:17408\nv_mfma_f32_16x16x32_bf16 v[72:75], a[32:35], v[244:247], v[72:75]\n" \
  "ds_read_b64_tr_b16 a[18:19], v56 offset:17920\nv_mfma_f32_16x16x32_bf16 v[76:79], a[32:35], v[248:251], v[76:79]\n" \
  "ds_read_b64_tr_b16 a[20:21], v57 offset:17408\nv_mfma_f32_16x16x32_bf16 v[80:83], a[32:35], v[252:255], v[80:83]\n" \
  "buffer_load_dwordx4 v[180:183], %[va], %[ra], %[k] offen offset:256 sc1\nv_mfma_f32_16x16x32_bf16 a[192:195], a[36:39], v[236:239], a[192:195]\n" \
  "ds_read_b64_tr_b16 a[22:23], v57 offset:17920\nv_mfma_f32_16x16x32_bf16 a[196:199], a[36:39], v[240:243], a[196:199]\n" \
  "ds_read_b64_tr_b16 a[24:25], v58 offset:17408\nv_mfma_f32_16x16x32_bf16 a[200:203], a[36:39], v[244:247], a[200:203]\n" \
  "ds_read_b64_tr_b16 a[26:27], v58 offset:17920\nv_mfma_f32_16x16x32_bf16 a[204:207], a[36:39], v[248:251], a[204:207]\n" \
  "buffer_load_dwordx4 v[184:187], %[va], %[ra], %[k] offen offset:512 sc1\nv_mfma_f32_16x16x32_bf16 a[208:211], a[36:39], v[252:255], a[208:211]\n" \
  "ds_read_b64_tr_b16 a[28:29], v59 offset:17408\nv_mfma_f32_16x16x32_bf16 a[212:215], a[40:43], v[236:239], a[212:215]\n" \
  "ds_read_b64_tr_b16 a[30:31], v59 offset:17920\nv_mfma_f32_16x16x32_bf16 a[216:219], a[40:43], v[240:243], a[216:219]\n" \
  "s_waitcnt vmcnt(15)\nv_max_u32 v60, v116, v118\n" \
  "v_mfma_f32_16x16x32_bf16 a[220:223], a[40:43], v[244:247], a[220:223]\nbuffer_load_dwordx4 v[188:191], %[va], %[ra], %[k] offen offset:768 sc1\n" \
  "v_mfma_f32_16x16x32_bf16 a[224:227], a[40:43], v[248:251], a[224:227]\nv_max3_u32 v60, v60, v120, v122\n" \
  "v_mfma_f32_16x16x32_bf16 a[228:231], a[40:43], v[252:255], a[228:231]\nv_max3_u32 v60, v60, v124, v126\n" \
  "v_mfma_f32_16x16x32_bf16 a[232:235], a[44:47], v[236:239], a[232:235]\nv_max3_u32 v60, v60, v128, v130\n" \
  "v_mfma_f32_16x16x32_bf16 a[236:239], a[44:47], v[240:243], a[236:239]\nbuffer_load_dwordx4 v[192:195], %[va], %[ra], %[k] offen offset:1024 sc1\n" \
  "s_add_u32 %[k], %[k], 5120\nv_mfma_f32_16x16x32_bf16 a[240:243], a[44:47], v[244:247], a[240:243]\n" \
  "v_max3_u32 v60, v60, v132, v134\nv_mfma_f32_16x16x32_bf16 a[244:247], a[44:47], v[248:251], a[244:247]\n" \
  "v_cmp_eq_u32 vcc, -1, v60\nv_mfma_f32_16x16x32_bf16 a[248:251], a[44:47], v[252:255], a[248:251]\n" \
  "s_cbranch_vccnz 107b\n207:\n" \
  "v_mfma_f32_16x16x32_bf16 v[64:67], a[48:51], v[116:119], v[64:67]\nbuffer_load_dwordx4 v[196:199], %[va], %[ra], %[k] offen offset:0 sc1\n" \
  "v_mfma_f32_16x16x32_bf16 v[68:71], a[48:51], v[120:123], v[68:71]\nds_read_b64_tr_b16 a[32:33], v56 offset:21760\n" \
  "v_mfma_f32_16x16x32_bf16 v[72:75], a[48:51], v[124:127], v[72:75]\nds_read_b64_tr_b16 a[34:35], v56 offset:22272\n" \
  "v_mfma_f32_16x16x32_bf16 v[76:79], a[48:51], v[128:131], v[76:79]\nds_read_b64_tr_b16 a[36:37], v57 offset:21760\n" \
  "v_mfma_f32_16x16x32_bf16 v[80:83], a[48:51], v[132:135], v[80:83]\nbuffer_load_dwordx4 v[200:203], %[va], %[ra], %[k] offen offset:256 sc1\n" \
  "v_mfma_f32_16x16x32_bf16 a[192:195], a[52:55], v[116:119], a[192:195]\nds_read_b64_tr_b16 a[38:39], v57 offset:22272\n" \
  "v_mfma_f32_16x16x32_bf16 a[196:199], a[52:55], v[120:123], a[196:199]\nds_read_b64_tr_b16 a[40:41], v58 offset:21760\n" \
  "v_mfma_f32_16x16x32_bf16 a[200:203], a[52:55], v[124:127], a[200:203]\nds_read_b64_tr_b16 a[42:43], v58 offset:22272\n" \
  "v_mfma_f32_16x16x32_bf16 a[204:207], a[52:55], v[128:131], a[204:207]\nbuffer_load_dwordx4 v[204:207], %[va], %[ra], %[k] offen offset:512 sc1\n" \
  "v_mfma_f32_16x16x32_bf16 a[208:211], a[52:55], v[132:135], a[208:211]\nds_read_b64_tr_b16 a[44:45], v59 offset:21760\n" \
  "v_mfma_f32_16x16x32_bf16 a[212:215], a[56:59], v[116:119], a[212:215]\nds_read_b64_tr_b16 a[46:47], v59 offset:22272\n" \
  "v_mfma_f32_16x16x32_bf16 a[216:219], a[56:59], v[120:123], a[216:219]\ns_waitcnt vmcnt(14)\n" \
  "v_max_u32 v60, v136, v138\nv_mfma_f32_16x16x32_bf16 a[220:223], a[56:59], v[124:127], a[220:223]\n" \
  "buffer_load_dwordx4 v[208:211], %[va], %[ra], %[k] offen offset:768 sc1\nv_mfma_f32_16x16x32_bf16 a[224:227], a[56:59], v[128:131], a[224:227]\n" \
  "v_max3_u32 v60, v60, v140, v142\nv_mfma_f32_16x16x32_bf16 a[228:231], a[56:59], v[132:135], a[228:231]\n" \
  "v_max3_u32 v60, v60, v144, v146\nv_mfma_f32_16x16x32_bf16 a[232:235], a[60:63], v[116:119], a[232:235]\n" \
  "v_max3_u32 v60, v60, v148, v150\nv_mfma_f32_16x16x32_bf16 a[236:239], a[60:63], v[120:123], a[236:239]\n" \
  "buffer_load_dwordx4 v[212:215], %[va], %[ra], %[k] offen offset:1024 sc1\ns_add_u32 %[k], %[k], 5120\n" \
  "v_mfma_f32_16x16x32_bf16 a[240:243], a[60:63], v[124:127], a[240:243]\nv_max3_u32 v60, v60, v152, v154\n" \
  "v_mfma_f32_16x16x32_bf16 a[244:247], a[60:63], v[128:131], a[244:247]\nv_cmp_eq_u32 vcc, -1, v60\n" \
  "v_mfma_f32_16x16x32_bf16 a[248:251], a[60:63], v[132:135], a[248:251]\ns_cbranch_vccnz 108b\n" \
  "208:\nv_mfma_f32_16x16x32_bf16 v[64:67], a[64:67], v[136:139], v[64:67]\n" \
  "buffer_load_dwordx4 v[216:219], %[va], %[ra], %[k] offen offset:0 sc1\nv_mfma_f32_16x16x32_bf16 v[68:71], a[64:67], v[140:143], v[68:71]\n" \
  "ds_read_b64_tr_b16 a[48:49], v56 offset:26112\nv_mfma_f32_16x16x32_bf16 v[72:75], a[64:67], v[144:147], v[72:75]\n" \
  "ds_read_b64_tr_b16 a[50:51], v56 offset:26624\nv_mfma_f32_16x16x32_bf16 v[76:79], a[64:67], v[148:151], v[76:79]\n" \
  "ds_read_b64_tr_b16 a[52:53], v57 offset:26112\nv_mfma_f32_16x16x32_bf16 v[80:83], a[64:67], v[152:155], v[80:83]\n" \
  "buffer_load_dwordx4 v[220:223], %[va], %[ra], %[k] offen offset:256 sc1\nv_mfma_f32_16x16x32_bf16 a[192:195], a[68:71], v[136:139], a[192:195]\n" \
  "ds_read_b64_tr_b16 a[54:55], v57 offset:26624\nv_mfma_f32_16x16x32_bf16 a[196:199], a[68:71], v[140:143], a[196:199]\n" \
  "ds_read_b64_tr_b16 a[56:57], v58 offset:26112\nv_mfma_f32_16x16x32_bf16 a[200:203], a[68:71], v[144:147], a[200:203]\n" \
  "ds_read_b64_tr_b16 a[58:59], v58 offset:26624\nv_mfma_f32_16x16x32_bf16 a[204:207], a[68:71], v[148:151], a[204:207]\n" \
  "buffer_load_dwordx4 v[224:227], %[va], %[ra], %[k] offen offset:512 sc1\nv_mfma_f32_16x16x32_bf16 a[208:211], a[68:71], v[152:155], a[208:211]\n" \
  "ds_read_b64_tr_b16 a[60:61], v59 offset:26112\nv_mfma_f32_16x16x32_bf16 a[212:215], a[72:75], v[136:139], a[212:215]\n" \
  "ds_read_b64_tr_b16 a[62:63], v59 offset:26624\nv_mfma_f32_16x16x32_bf16 a[216:219], a[72:75], v[140:143], a[216:219]\n" \
  "s_waitcnt vmcnt(13)\nv_max_u32 v60, v156, v158\n" \
  "v_mfma_f32_16x16x32_bf16 a[220:223], a[72:75], v[144:147], a[220:223]\nbuffer_load_dwordx4 v[228:231], %[va], %[ra], %[k] offen offset:768 sc1\n" \
  "v_mfma_f32_16x16x32_bf16 a[224:227], a[72:75], v[148:151], a[224:227]\nv_max3_u32 v60, v60, v160, v162\n" \
  "v_mfma_f32_16x16x32_bf16 a[228:231], a[72:75], v[152:155], a[228:231]\nv_max3_u32 v60, v60, v164, v166\n" \
  "v_mfma_f32_16x16x32_bf16 a[232:235], a[76:79], v[136:139], a[232:235]\nv_max3_u32 v60, v60, v168, v170\n" \
  "v_mfma_f32_16x16x32_bf16 a[236:239], a[76:79], v[140:143], a[236:239]\nbuffer_load_dwordx4 v[232:235], %[va], %[ra], %[k] offen offset:1024 sc1\n" \
  "s_add_u32 %[k], %[k], 5120\nv_mfma_f32_16x16x32_bf16 a[240:243], a[76:79], v[144:147], a[240:243]\n" \
  "v_max3_u32 v60, v60, v172, v174\nv_mfma_f32_16x16x32_bf16 a[244:247], a[76:79], v[148:151], a[244:247]\n" \
  "v_cmp_eq_u32 vcc, -1, v60\nv_mfma_f32_16x16x32_bf16 a[248:251], a[76:79], v[152:155], a[248:251]\n" \
  "s_cbranch_vccnz 109b\n209:\n" \
  "s_waitcnt vmcnt(18)\nv_mfma_f32_16x16x32_bf16 v[64:67], a[80:83], v[156:159], v[64:67]\n" \
  "buffer_load_dwordx4 v[236:239], %[va], %[ra], %[k] offen offset:0 sc1\nv_mfma_f32_16x16x32_bf16 v[68:71], a[80:83], v[160:163], v[68:71]\n" \
  "ds_read_b64_tr_b16 a[64:65], v56 offset:0\nv_mfma_f32_16x16x32_bf16 v[72:75], a[80:83], v[164:167], v[72:75]\n" \
  "ds_read_b64_tr_b16 a[66:67], v56 offset:512\nv_mfma_f32_16x16x32_bf16 v[76:79], a[80:83], v[168:171], v[76:79]\n" \
  "ds_read_b64_tr_b16 a[68:69], v57 offset:0\nv_mfma_f32_16x16x32_bf16 v[80:83], a[80:83], v[172:175], v[80:83]\n" \
  "buffer_load_dwordx4 v[240:243], %[va], %[ra], %[k] offen offset:256 sc1\nv_mfma_f32_16x16x32_bf16 a[192:195], a[84:87], v[156:159], a[192:195]\n" \
  "ds_read_b64_tr_b16 a[70:71], v57 offset:512\nv_mfma_f32_16x16x32_bf16 a[196:199], a[84:87], v[160:163], a[196:199]\n" \
  "ds_read_b64_tr_b16 a[72:73], v58 offset:0\nv_mfma_f32_16x16x32_bf16 a[200:203], a[84:87], v[164:167], a[200:203]\n" \
  "ds_read_b64_tr_b16 a[74:75], v58 offset:512\nv_mfma_f32_16x16x32_bf16 a[204:207], a[84:87], v[168:171], a[204:207]\n" \
  "buffer_load_dwordx4 v[244:247], %[va], %[ra], %[k] offen offset:512 sc1\nv_mfma_f32_16x16x32_bf16 a[208:211], a[84:87], v[172:175], a[208:211]\n" \
  "ds_read_b64_tr_b16 a[76:77], v59 offset:0\nv_mfma_f32_16x16x32_bf16 a[212:215], a[88:91], v[156:159], a[212:215]\n" \
  "ds_read_b64_tr_b16 a[78:79], v59 offset:512\nv_mfma_f32_16x16x32_bf16 a[216:219], a[88:91], v[160:163], a[216:219]\n" \
  "s_waitcnt vmcnt(13)\nv_max_u32 v60, v176, v178\n" \
  "v_mfma_f32_16x16x32_bf16 a[220:223], a[88:91], v[164:167], a[220:223]\nbuffer_load_dwordx4 v[248:251], %[va], %[ra], %[k] offen offset:768 sc1\n" \
  "v_mfma_f32_16x16x32_bf16 a[224:227], a[88:91], v[168:171], a[224:227]\nv_max3_u32 v60, v60, v180, v182\n" \
  "v_mfma_f32_16x16x32_bf16 a[228:231], a[88:91], v[172:175], a[228:231]\nv_max3_u32 v60, v60, v184, v186\n" \
  "v_mfma_f32_16x16x32_bf16 a[232:235], a[92:95], v[156:159], a[232:235]\nv_max3_u32 v60, v60, v188, v190\n" \
  "v_mfma_f32_16x16x32_bf16 a[236:239], a[92:95], v[160:163], a[236:239]\nbuffer_load_dwordx4 v[252:255], %[va], %[ra], %[k] offen offset:1024 sc1\n" \
  "s_add_u32 %[k], %[k], 5120\nv_mfma_f32_16x16x32_bf16 a[240:243], a[92:95], v[164:167], a[240:243]\n" \
  "v_max3_u32 v60, v60, v192, v194\nv_mfma_f32_16x16x32_bf16 a[244:247], a[92:95], v[168:171], a[244:247]\n" \
  "v_cmp_eq_u32 vcc, -1, v60\nv_mfma_f32_16x16x32_bf16 a[248:251], a[92:95], v[172:175], a[248:251]\n" \
  "s_cbranch_vccnz 110b\n210:\n" \
  "s_waitcnt lgkmcnt(0)\ns_barrier\n" \
  "v_mfma_f32_16x16x32_bf16 a[192:195], a[100:103], v[176:179], a[192:195]\nbuffer_load_dwordx4 v[116:119], %[va], %[ra], %[k] offen offset:0 sc1\n" \
  "v_mfma_f32_16x16x32_bf16 a[196:199], a[100:103], v[180:183], a[196:199]\nv_mfma_f32_16x16x32_bf16 a[200:203], a[100:103], v[184:187], a[200:203]\n" \
  "v_mfma_f32_16x16x32_bf16 a[204:207], a[100:103], v[188:191], a[204:207]\nbuffer_load_dwordx4 v[120:123], %[va], %[ra], %[k] offen offset:256 sc1\n" \
  "v_mfma_f32_16x16x32_bf16 a[208:211], a[100:103], v[192:195], a[208:211]\nv_mfma_f32_16x16x32_bf16 a[212:215], a[104:107], v[176:179], a[212:215]\n" \
  "v_mfma_f32_16x16x32_bf16 a[216:219], a[104:107], v[180:183], a[216:219]\nbuffer_load_dwordx4 v[124:127], %[va], %[ra], %[k] offen offset:512 sc1\n" \
  "v_mfma_f32_16x16x32_bf16 a[220:223], a[104:107], v[184:187], a[220:223]\ns_waitcnt vmcnt(13)\n" \
  "v_max_u32 v60, v196, v198\nv_mfma_f32_16x16x32_bf16 a[224:227], a[104:107], v[188:191], a[224:227]\n" \
  "v_max3_u32 v60, v60, v200, v202\nv_mfma_f32_16x16x32_bf16 a[228:231], a[104:107], v[192:195], a[228:231]\n" \
  "buffer_load_dwordx4 v[128:131], %[va], %[ra], %[k] offen offset:768 sc1\nv_mfma_f32_16x16x32_bf16 a[232:235], a[108:111], v[176:179], a[232:235]\n" \
  "v_max3_u32 v60, v60, v204, v206\nv_mfma_f32_16x16x32_bf16 a[236:239], a[108:111], v[180:183], a[236:239]\n" \
  "v_max3_u32 v60, v60, v208, v210\nv_mfma_f32_16x16x32_bf16 a[240:243], a[108:111], v[184:187], a[240:243]\n" \
  "buffer_load_dwordx4 v[132:135], %[va], %[ra], %[k] offen offset:1024 sc1\ns_add_u32 %[k], %[k], 5120\n" \
  "v_mfma_f32_16x16x32_bf16 a[244:247], a[108:111], v[188:191], a[244:247]\nv_max3_u32 v60, v60, v212, v214\n" \
  "v_mfma_f32_16x16x32_bf16 a[248:251], a[108:111], v[192:195], a[248:251]\nv_cmp_eq_u32 vcc, -1, v60\n" \
  "s_cbranch_vccnz 111b\n211:\n" \
  "v_mfma_f32_16x16x32_bf16 a[192:195], a[116:119], v[196:199], a[192:195]\nbuffer_load_dwordx4 v[136:139], %[va], %[ra], %[k] offen offset:0 sc1\n" \
  "v_mfma_f32_16x16x32_bf16 a[196:199], a[116:119], v[200:203], a[196:199]\nv_mfma_f32_16x16x32_bf16 a[200:203], a[116:119], v[204:207], a[200:203]\n" \
  "v_mfma_f32_16x16x32_bf16 a[204:207], a[116:119], v[208:211], a[204:207]\nbuffer_load_dwordx4 v[140:143], %[va], %[ra], %[k] offen offset:256 sc1\n" \
  "v_mfma_f32_16x16x32_bf16 a[208:211], a[116:119], v[212:215], a[208:211]\nv_mfma_f32_16x16x32_bf16 a[212:215], a[120:123], v[196:199], a[212:215]\n" \
  "v_mfma_f32_16x16x32_bf16 a[216:219], a[120:123], v[200:203], a[216:219]\nbuffer_load_dwordx4 v[144:147], %[va], %[ra], %[k] offen offset:512 sc1\n" \
  "v_mfma_f32_16x16x32_bf16 a[220:223], a[120:123], v[204:207], a[220:223]\ns_waitcnt vmcnt(13)\n" \
  "v_max_u32 v60, v216, v218\nv_mfma_f32_16x16x32_bf16 a[224:227], a[120:123], v[208:211], a[224:227]\n" \
  "v_max3_u32 v60, v60, v220, v222\nv_mfma_f32_16x16x32_bf16 a[228:231], a[120:123], v[212:215], a[228:231]\n" \
  "buffer_load_dwordx4 v[148:151], %[va], %[ra], %[k] offen offset:768 sc1\nv_mfma_f32_16x16x32_bf16 a[232:235], a[124:127], v[196:199], a[232:235]\n" \
  "v_max3_u32 v60, v60, v224, v226\nv_mfma_f32_16x16x32_bf16 a[236:239], a[124:127], v[200:203], a[236:239]\n" \
  "v_max3_u32 v60, v60, v228, v230\nv_mfma_f32_16x16x32_bf16 a[240:243], a[124:127], v[204:207], a[240:243]\n" \
  "buffer_load_dwordx4 v[152:155], %[va], %[ra], %[k] offen offset:1024 sc1\ns_add_u32 %[k], %[k], 5120\n" \
  "v_mfma_f32_16x16x32_bf16 a[244:247], a[124:127], v[208:211], a[244:247]\nv_max3_u32 v60, v60, v232, v234\n" \
  "v_mfma_f32_16x16x32_bf16 a[248:251], a[124:127], v[212:215], a[248:251]\nv_cmp_eq_u32 vcc, -1, v60\n" \
  "s_cbranch_vccnz 112b\n212:\n" \
  "v_mfma_f32_16x16x32_bf16 a[192:195], a[132:135], v[216:219], a[192:195]\ns_mov_b32 m0, %[l]\n" \
  "s_mov_b32 %[t], %[sw2]\nbuffer_load_dwordx4 %[vw2], %[rw2], %[t] offen lds\n" \
  "s_add_u32 m0, m0, 1088\ns_add_u32 %[t], %[t], 32768\n" \
  "v_mfma_f32_16x16x32_bf16 a[196:199], a[132:135], v[220:223], a[196:199]\nv_mfma_f32_16x16x32_bf16 a[200:203], a[132:135], v[224:227], a[200:203]\n" \
  "v_mfma_f32_16x16x32_bf16 a[204:207], a[132:135], v[228:231], a[204:207]\nbuffer_load_dwordx4 %[vw2], %[rw2], %[t] offen lds\n" \
  "s_add_u32 m0, m0, 1088\ns_add_u32 %[t], %[t], 32768\n" \
  "v_mfma_f32_16x16x32_bf16 a[208:211], a[132:135], v[232:235], a[208:211]\nv_mfma_f32_16x16x32_bf16 a[212:215], a[136:139], v[216:219], a[212:215]\n" \
  "v_mfma_f32_16x16x32_bf16 a[216:219], a[136:139], v[220:223], a[216:219]\nbuffer_load_dwordx4 %[vw2], %[rw2], %[t] offen lds\n" \
  "s_add_u32 m0, m0, 1088\ns_add_u32 %[t], %[t], 32768\n" \
  "v_mfma_f32_16x16x32_bf16 a[220:223], a[136:139], v[224:227], a[220:223]\ns_waitcnt vmcnt(13)\n" \
  "v_max_u32 v60, v236, v238\nv_mfma_f32_16x16x32_bf16 a[224:227], a[136:139], v[228:231], a[224:227]\n" \
  "v_max3_u32 v60, v60, v240, v242\nv_mfma_f32_16x16x32_bf16 a[228:231], a[136:139], v[232:235], a[228:231]\n" \
  "buffer_load_dwordx4 %[vw2], %[rw2], %[t] offen lds\ns_add_u32 m0, m0, 1088\n" \
  "s_add_u32 %[t], %[t], 32768\nv_mfma_f32_16x16x32_bf16 a[232:235], a[140:143], v[216:219], a[232:235]\n" \
  "v_max3_u32 v60, v60, v244, v246\nv_mfma_f32_16x16x32_bf16 a[236:239], a[140:143], v[220:223], a[236:239]\n" \
  "v_max3_u32 v60, v60, v248, v250\nv_mfma_f32_16x16x32_bf16 a[240:243], a[140:143], v[224:227], a[240:243]\n" \
  "buffer_load_dwordx4 %[vw2], %[rw2], %[t] offen lds\ns_add_u32 m0, m0, 1088\n" \
  "s_add_u32 %[t], %[t], 32768\nv_mfma_f32_16x16x32_bf16 a[244:247], a[140:143], v[228:231], a[244:247]\n" \
  "v_max3_u32 v60, v60, v252, v254\nv_mfma_f32_16x16x32_bf16 a[248:251], a[140:143], v[232:235], a[248:251]\n" \
  "v_cmp_eq_u32 vcc, -1, v60\ns_cbranch_vccnz 113b\n" \
  "213:\nv_mfma_f32_16x16x32_bf16 a[192:195], a[148:151], v[236:239], a[192:195]\n" \
  "buffer_load_dwordx4 %[vw2], %[rw2], %[t] offen lds\ns_add_u32 m0, m0, 1088\n" \
  "s_add_u32 %[t], %[t], 32768\nv_mfma_f32_16x16x32_bf16 a[196:199], a[148:151], v[240:243], a[196:199]\n" \
  "v_mfma_f32_16x16x32_bf16 a[200:203], a[148:151], v[244:247], a[200:203]\nv_mfma_f32_16x16x32_bf16 a[204:207], a[148:151], v[248:251], a[204:207]\n" \
  "buffer_load_dwordx4 %[vw2], %[rw2], %[t] offen lds\ns_add_u32 m0, m0, 1088\n" \
  "s_add_u32 %[t], %[t], 32768\nv_mfma_f32_16x16x32_bf16 a[208:211], a[148:151], v[252:255], a[208:211]\n" \
  "v_mfma_f32_16x16x32_bf16 a[212:215], a[152:155], v[236:239], a[212:215]\nv_mfma_f32_16x16x32_bf16 a[216:219], a[152:155], v[240:243], a[216:219]\n" \
  "buffer_load_dwordx4 %[vw2], %[rw2], %[t] offen lds\ns_add_u32 m0, m0, 1088\n" \
  "s_add_u32 %[t], %[t], 32768\nv_mfma_f32_16x16x32_bf16 a[220:223], a[152:155], v[244:247], a[220:223]\n" \
  "s_waitcnt vmcnt(13)\nv_max_u32 v60, v116, v118\n" \
  "v_mfma_f32_16x16x32_bf16 a[224:227], a[152:155], v[248:251], a[224:227]\nv_max3_u32 v60, v60, v120, v122\n" \
  "v_mfma_f32_16x16x32_bf16 a[228:231], a[152:155], v[252:255], a[228:231]\nbuffer_load_dwordx4 %[vw2], %[rw2], %[t] offen lds\n" \
  "s_add_u32 m0, m0, 1088\ns_add_u32 %[t], %[t], 32768\n" \
  "v_mfma_f32_16x16x32_bf16 a[232:235], a[156:159], v[236:239], a[232:235]\nv_max3_u32 v60, v60, v124, v126\n" \
  "v_mfma_f32_16x16x32_bf16 a[236:239], a[156:159], v[240:243], a[236:239]\nv_max3_u32 v60, v60, v128, v130\n" \
  "v_mfma_f32_16x16x32_bf16 a[240:243], a[156:159], v[244:247], a[240:243]\nbuffer_load_dwordx4 %[vw2], %[rw2], %[t] offen lds\n" \
  "s_add_u32 m0, m0, 1088\ns_add_u32 %[t], %[t], 32768\n" \
  "v_mfma_f32_16x16x32_bf16 a[244:247], a[156:159], v[248:251], a[244:247]\nv_max3_u32 v60, v60, v132, v134\n" \
  "v_mfma_f32_16x16x32_bf16 a[248:251], a[156:159], v[252:255], a[248:251]\nv_cmp_eq_u32 vcc, -1, v60\n" \
  "s_cbranch_vccnz 114b\n214:\n" \
  "v_mfma_f32_16x16x32_bf16 a[192:195], a[164:167], v[116:119], a[192:195]\nbuffer_load_dwordx4 %[vw2], %[rw2], %[t] offen lds\n" \
  "s_add_u32 m0, m0, 1088\ns_add_u32 %[t], %[t], 32768\n" \
  "v_mfma_f32_16x16x32_bf16 a[196:199], a[164:167], v[120:123], a[196:199]\nv_mfma_f32_16x16x32_bf16 a[200:203], a[164:167], v[124:127], a[200:203]\n" \
  "v_mfma_f32_16x16x32_bf16 a[204:207], a[164:167], v[128:131], a[204:207]\nbuffer_load_dwordx4 %[vw2], %[rw2], %[t] offen lds\n" \
  "s_add_u32 m0, m0, 1088\ns_add_u32 %[t], %[t], 32768\n" \
  "v_mfma_f32_16x16x32_bf16 a[208:211], a[164:167], v[132:135], a[208:211]\nv_mfma_f32_16x16x32_bf16 a[212:215], a[168:171], v[116:119], a[212:215]\n" \
  "v_mfma_f32_16x16x32_bf16 a[216:219], a[168:171], v[120:123], a[216:219]\nbuffer_load_dwordx4 %[vw2], %[rw2], %[t] offen lds\n" \
  "s_add_u32 m0, m0, 1088\ns_add_u32 %[t], %[t], 32768\n" \
  "v_mfma_f32_16x16x32_bf16 a[220:223], a[168:171], v[124:127], a[220:223]\ns_waitcnt vmcnt(13)\n" \
  "v_max_u32 v60, v136, v138\nv_mfma_f32_16x16x32_bf16 a[224:227], a[168:171], v[128:131], a[224:227]\n" \
  "v_max3_u32 v60, v60, v140, v142\nv_mfma_f32_16x16x32_bf16 a[228:231], a[168:171], v[132:135], a[228:231]\n" \
  "buffer_load_dwordx4 %[vw2], %[rw2], %[t] offen lds\ns_add_u32 m0, m0, 1088\n" \
  "s_add_u32 %[t], %[t], 32768\nv_mfma_f32_16x16x32_bf16 a[232:235], a[172:175], v[116:119], a[232:235]\n" \
  "v_max3_u32 v60, v60, v144, v146\nv_mfma_f32_16x16x32_bf16 a[236:239], a[172:175], v[120:123], a[236:239]\n" \
  "v_max3_u32 v60, v60, v148, v150\nv_mfma_f32_16x16x32_bf16 a[240:243], a[172:175], v[124:127], a[240:243]\n" \
  "buffer_load_dwordx4 %[vw2], %[rw2], %[t] offen lds\ns_add_u32 m0, m0, 1088\n" \
  "s_add_u32 %[t], %[t], 32768\nv_mfma_f32_16x16x32_bf16 a[244:247], a[172:175], v[128:131], a[244:247]\n" \
  "v_max3_u32 v60, v60, v152, v154\nv_mfma_f32_16x16x32_bf16 a[248:251], a[172:175], v[132:135], a[248:251]\n" \
  "v_cmp_eq_u32 vcc, -1, v60\ns_cbranch_vccnz 115b\n" \
  "215:\nv_mfma_f32_16x16x32_bf16 a[192:195], a[180:183], v[136:139], a[192:195]\n" \
  "buffer_load_dwordx4 %[vw2], %[rw2], %[t] offen lds\ns_add_u32 m0, m0, 1088\n" \
  "s_add_u32 %[t], %[t], 32768\nv_mfma_f32_16x16x32_bf16 a[196:199], a[180:183], v[140:143], a[196:199]\n" \
  "v_mfma_f32_16x16x32_bf16 a[200:203], a[180:183], v[144:147], a[200:203]\nv_mfma_f32_16x16x32_bf16 a[204:207], a[180:183], v[148:151], a[204:207]\n" \
  "v_mfma_f32_16x16x32_bf16 a[208:211], a[180:183], v[152:155], a[208:211]\ns_add_u32 m0, %[l], 34816\n" \
  "buffer_load_dwordx4 %[vw2], %[rw2], %[t] offen lds\ns_add_u32 m0, m0, 1088\n" \
  "s_add_u32 %[t], %[t], 32768\nv_mfma_f32_16x16x32_bf16 a[212:215], a[184:187], v[136:139], a[212:215]\n" \
  "v_mfma_f32_16x16x32_bf16 a[216:219], a[184:187], v[140:143], a[216:219]\nv_mfma_f32_16x16x32_bf16 a[220:223], a[184:187], v[144:147], a[220:223]\n" \
  "v_mfma_f32_16x16x32_bf16 a[224:227], a[184:187], v[148:151], a[224:227]\nbuffer_load_dwordx4 %[vw2], %[rw2], %[t] offen lds\n" \
  "s_add_u32 m0, m0, 1088\ns_add_u32 %[t], %[t], 32768\n" \
  "ds_write_b128 v61, a[192:195] offset:0\nv_mfma_f32_16x16x32_bf16 a[228:231], a[184:187], v[152:155], a[228:231]\n" \
  "v_mfma_f32_16x16x32_bf16 a[232:235], a[188:191], v[136:139], a[232:235]\nds_write_b128 v61, a[196:199] offset:1024\n" \
  "v_mfma_f32_16x16x32_bf16 a[236:239], a[188:191], v[140:143], a[236:239]\nv_mfma_f32_16x16x32_bf16 a[240:243], a[188:191], v[144:147], a[240:243]\n" \
  "buffer_load_dwordx4 %[vw2], %[rw2], %[t] offen lds\ns_add_u32 m0, m0, 1088\n" \
  "s_add_u32 %[t], %[t], 32768\nds_write_b128 v61, a[200:203] offset:2048\n" \
  "v_mfma_f32_16x16x32_bf16 a[244:247], a[188:191], v[148:151], a[244:247]\nv_mfma_f32_16x16x32_bf16 a[248:251], a[188:191], v[152:155], a[248:251]\n" \
  "ds_write_b128 v61, a[204:207] offset:3072\nv_mfma_f32_16x16x32_bf16 v[64:67], a[96:99], v[176:179], v[64:67]\n" \
  "v_mfma_f32_16x16x32_bf16 v[68:71], a[96:99], v[180:183], v[68:71]\nbuffer_load_dwordx4 %[vw2], %[rw2], %[t] offen lds\n" \
  "s_add_u32 m0, m0, 1088\ns_add_u32 %[t], %[t], 32768\n" \
  "ds_write_b128 v61, a[208:211] offset:4096\nv_mfma_f32_16x16x32_bf16 v[72:75], a[96:99], v[184:187], v[72:75]\n" \
  "v_mfma_f32_16x16x32_bf16 v[76:79], a[96:99], v[188:191], v[76:79]\nds_write_b128 v62, a[212:215] offset:5120\n" \
  "v_mfma_f32_16x16x32_bf16 v[80:83], a[96:99], v[192:195], v[80:83]\nv_mfma_f32_16x16x32_bf16 v[64:67], a[112:115], v[196:199], v[64:67]\n" \
  "ds_write_b128 v62, a[216:219] offset:6144\nv_mfma_f32_16x16x32_bf16 v[68:71], a[112:115], v[200:203], v[68:71]\n" \
  "v_mfma_f32_16x16x32_bf16 v[72:75], a[112:115], v[204:207], v[72:75]\nds_write_b128 v62, a[220:223] offset:7168\n" \
  "v_mfma_f32_16x16x32_bf16 v[76:79], a[112:115], v[208:211], v[76:79]\nv_mfma_f32_16x16x32_bf16 v[80:83], a[112:115], v[212:215], v[80:83]\n" \
  "ds_write_b128 v62, a[224:227] offset:8192\nv_mfma_f32_16x16x32_bf16 v[64:67], a[128:131], v[216:219], v[64:67]\n" \
  "v_mfma_f32_16x16x32_bf16 v[68:71], a[128:131], v[220:223], v[68:71]\nds_write_b128 v62, a[228:231] offset:9216\n" \
  "v_mfma_f32_16x16x32_bf16 v[72:75], a[128:131], v[224:227], v[72:75]\nv_mfma_f32_16x16x32_bf16 v[76:79], a[128:131], v[228:231], v[76:79]\n" \
  "ds_write_b128 v63, a[232:235] offset:10240\nv_mfma_f32_16x16x32_bf16 v[80:83], a[128:131], v[232:235], v[80:83]\n" \
  "v_mfma_f32_16x16x32_bf16 v[64:67], a[144:147], v[236:239], v[64:67]\nds_write_b128 v63, a[236:239] offset:11264\n" \
  "v_mfma_f32_16x16x32_bf16 v[68:71], a[144:147], v[240:243], v[68:71]\nv_mfma_f32_16x16x32_bf16 v[72:75], a[144:147], v[244:247], v[72:75]\n" \
  "ds_write_b128 v63, a[240:243] offset:12288\nv_mfma_f32_16x16x32_bf16 v[76:79], a[144:147], v[248:251], v[76:79]\n" \
  "v_mfma_f32_16x16x32_bf16 v[80:83], a[144:147], v[252:255], v[80:83]\nds_write_b128 v63, a[244:247] offset:13312\n" \
  "v_mfma_f32_16x16x32_bf16 v[64:67], a[160:163], v[116:119], v[64:67]\nv_mfma_f32_16x16x32_bf16 v[68:71], a[160:163], v[120:123], v[68:71]\n" \
  "ds_write_b128 v63, a[248:251] offset:14336\ns_mul_i32 %[u], %[wv], 40960\n" \
  "v_add_u32 v60, %[u], %[vl]\nv_mfma_f32_16x16x32_bf16 v[72:75], a[160:163], v[124:127], v[72:75]\n" \
  "v_mfma_f32_16x16x32_bf16 v[76:79], a[160:163], v[128:131], v[76:79]\nv_mfma_f32_16x16x32_bf16 v[80:83], a[160:163], v[132:135], v[80:83]\n" \
  "s_waitcnt lgkmcnt(0)\ns_barrier\n" \
  "ds_read_b128 v[84:87], v60 offset:0\nv_mfma_f32_16x16x32_bf16 v[64:67], a[176:179], v[136:139], v[64:67]\n" \
  "ds_read_b128 v[88:91], v60 offset:5120\nv_mfma_f32_16x16x32_bf16 v[68:71], a[176:179], v[140:143], v[68:71]\n" \
  "ds_read_b128 v[92:95], v60 offset:10240\nv_mfma_f32_16x16x32_bf16 v[72:75], a[176:179], v[144:147], v[72:75]\n" \
  "ds_read_b128 v[96:99], v60 offset:1024\nv_mfma_f32_16x16x32_bf16 v[76:79], a[176:179], v[148:151], v[76:79]\n" \
  "ds_read_b128 v[100:103], v60 offset:6144\nv_mfma_f32_16x16x32_bf16 v[80:83], a[176:179], v[152:155], v[80:83]\n" \
  "ds_read_b128 v[104:107], v60 offset:11264\nds_read_b128 v[108:111], v60 offset:2048\n" \
  "ds_read_b128 v[112:115], v60 offset:7168\nds_read_b128 v[156:159], v60 offset:12288\n" \
  "ds_read_b128 v[160:163], v60 offset:3072\nds_read_b128 v[164:167], v60 offset:8192\n" \
  "ds_read_b128 v[168:171], v60 offset:13312\nds_read_b128 v[172:175], v60 offset:4096\n" \
  "ds_read_b128 v[176:179], v60 offset:9216\nds_read_b128 v[180:183], v60 offset:14336\n" \
  "s_nop 3\ns_waitcnt lgkmcnt(12)\n" \
  "v_pk_add_f32 v[84:85], v[84:85], v[88:89]\nv_pk_add_f32 v[86:87], v[86:87], v[90:91]\n" \
  "v_pk_add_f32 v[64:65], v[64:65], v[92:93]\nv_pk_add_f32 v[66:67], v[66:67], v[94:95]\n" \
  "v_pk_add_f32 v[64:65], v[64:65], v[84:85]\nv_pk_add_f32 v[66:67], v[66:67], v[86:87]\n" \
  "s_waitcnt lgkmcnt(9)\nv_pk_add_f32 v[96:97], v[96:97], v[100:101]\n" \
  "v_pk_add_f32 v[98:99], v[98:99], v[102:103]\nv_pk_add_f32 v[68:69], v[68:69], v[104:105]\n" \
  "v_pk_add_f32 v[70:71], v[70:71], v[106:107]\nv_pk_add_f32 v[68:69], v[68:69], v[96:97]\n" \
  "v_pk_add_f32 v[70:71], v[70:71], v[98:99]\ns_waitcnt lgkmcnt(6)\n" \
  "v_pk_add_f32 v[108:109], v[108:109], v[112:113]\nv_pk_add_f32 v[110:111], v[110:111], v[114:115]\n" \
  "v_pk_add_f32 v[72:73], v[72:73], v[156:157]\nv_pk_add_f32 v[74:75], v[74:75], v[158:159]\n" \
  "v_pk_add_f32 v[72:73], v[72:73], v[108:109]\nv_pk_add_f32 v[74:75], v[74:75], v[110:111]\n" \
  "s_waitcnt lgkmcnt(3)\nv_pk_add_f32 v[160:161], v[160:161], v[164:165]\n" \
  "v_pk_add_f32 v[162:163], v[162:163], v[166:167]\nv_pk_add_f32 v[76:77], v[76:77], v[168:169]\n" \
  "v_pk_add_f32 v[78:79], v[78:79], v[170:171]\nv_pk_add_f32 v[76:77], v[76:77], v[160:161]\n" \
  "v_pk_add_f32 v[78:79], v[78:79], v[162:163]\ns_waitcnt lgkmcnt(0)\n" \
  "v_pk_add_f32 v[172:173], v[172:173], v[176:177]\nv_pk_add_f32 v[174:175], v[174:175], v[178:179]\n" \
  "v_pk_add_f32 v[80:81], v[80:81], v[180:181]\nv_pk_add_f32 v[82:83], v[82:83], v[182:183]\n" \
  "v_pk_add_f32 v[80:81], v[80:81], v[172:173]\nv_pk_add_f32 v[82:83], v[82:83], v[174:175]\n" \
  ""
#define G2_ASM \
  "s_movk_i32 %[k], 96\nv_xad_u32 v56, %[xc], 0, %[rb]\n" \
  "v_xad_u32 v57, %[xc], 32, %[rb]\nv_xad_u32 v58, %[xc], 64, %[rb]\n" \
  "v_xad_u32 v59, %[xc], %[k], %[rb]\ns_mov_b32 %[n], 262144\n" \
  "s_add_u32 m0, %[l], 17408\ns_add_u32 %[t], %[sw2], 655360\n" \
  "s_movk_i32 %[k], 16\n1:\n" \
  "buffer_load_dwordx4 %[vw2], %[rw2], %[t] offen lds\ns_add_u32 m0, m0, 1088\n" \
  "s_add_u32 %[t], %[t], 32768\ns_sub_u32 %[k], %[k], 1\n" \
  "s_cmp_lg_u32 %[k], 0\ns_cbranch_scc1 1b\n" \
  "s_branch 3f\n100:\n" \
  "s_sub_u32 %[n], %[n], 1\ns_cbranch_scc1 300f\n" \
  "s_add_u32 %[u], %[sa], 0\nbuffer_load_dwordx4 v[116:119], %[va], %[ra], %[u] offen offset:0 sc1\n" \
  "buffer_load_dwordx4 v[120:123], %[va], %[ra], %[u] offen offset:256 sc1\nbuffer_load_dwordx4 v[124:127], %[va], %[ra], %[u] offen offset:512 sc1\n" \
  "buffer_load_dwordx4 v[128:131], %[va], %[ra], %[u] offen offset:768 sc1\nbuffer_load_dwordx4 v[132:135], %[va], %[ra], %[u] offen offset:1024 sc1\n" \
  "s_waitcnt vmcnt(0)\nv_max_u32 v60, v116, v118\n" \
  "v_max3_u32 v60, v60, v120, v122\nv_max3_u32 v60, v60, v124, v126\n" \
  "v_max3_u32 v60, v60, v128, v130\nv_max3_u32 v60, v60, v132, v134\n" \
  "v_cmp_eq_u32 vcc, -1, v60\ns_cbranch_vccnz 100b\n" \
  "s_branch 200f\n300:\n" \
  "s_mov_b32 %[n], 0\ns_branch 200f\n" \
  "101:\ns_sub_u32 %[n], %[n], 1\n" \
  "s_cbranch_scc1 301f\ns_add_u32 %[u], %[sa], 5120\n" \
  "buffer_load_dwordx4 v[136:139], %[va], %[ra], %[u] offen offset:0 sc1\nbuffer_load_dwordx4 v[140:143], %[va], %[ra], %[u] offen offset:256 sc1\n" \
  "buffer_load_dwordx4 v[144:147], %[va], %[ra], %[u] offen offset:512 sc1\nbuffer_load_dwordx4 v[148:151], %[va], %[ra], %[u] offen offset:768 sc1\n" \
  "buffer_load_dwordx4 v[152:155], %[va], %[ra], %[u] offen offset:1024 sc1\ns_waitcnt vmcnt(0)\n" \
  "v_max_u32 v60, v136, v138\nv_max3_u32 v60, v60, v140, v142\n" \
  "v_max3_u32 v60, v60, v144, v146\nv_max3_u32 v60, v60, v148, v150\n" \
  "v_max3_u32 v60, v60, v152, v154\nv_cmp_eq_u32 vcc, -1, v60\n" \
  "s_cbranch_vccnz 101b\ns_branch 201f\n" \
  "301:\ns_mov_b32 %[n], 0\n" \
  "s_branch 201f\n102:\n" \
  "s_sub_u32 %[n], %[n], 1\ns_cbranch_scc1 302f\n" \
  "s_add_u32 %[u], %[sa], 10240\nbuffer_load_dwordx4 v[156:159], %[va], %[ra], %[u] offen offset:0 sc1\n" \
  "buffer_load_dwordx4 v[160:163], %[va], %[ra], %[u] offen offset:256 sc1\nbuffer_load_dwordx4 v[164:167], %[va], %[ra], %[u] offen offset:512 sc1\n" \
  "buffer_load_dwordx4 v[168:171], %[va], %[ra], %[u] offen offset:768 sc1\nbuffer_load_dwordx4 v[172:175], %[va], %[ra], %[u] offen offset:1024 sc1\n" \
  "s_waitcnt vmcnt(0)\nv_max_u32 v60, v156, v158\n" \
  "v_max3_u32 v60, v60, v160, v162\nv_max3_u32 v60, v60, v164, v166\n" \
  "v_max3_u32 v60, v60, v168, v170\nv_max3_u32 v60, v60, v172, v174\n" \
  "v_cmp_eq_u32 vcc, -1, v60\ns_cbranch_vccnz 102b\n" \
  "s_branch 202f\n302:\n" \
  "s_mov_b32 %[n], 0\ns_branch 202f\n" \
  "103:\ns_sub_u32 %[n], %[n], 1\n" \
  "s_cbranch_scc1 303f\ns_add_u32 %[u], %[sa], 15360\n" \
  "buffer_load_dwordx4 v[176:179], %[va], %[ra], %[u] offen offset:0 sc1\nbuffer_load_dwordx4 v[180:183], %[va], %[ra], %[u] offen offset:256 sc1\n" \
  "buffer_load_dwordx4 v[184:187], %[va], %[ra], %[u] offen offset:512 sc1\nbuffer_load_dwordx4 v[188:191], %[va], %[ra], %[u] offen offset:768 sc1\n" \
  "buffer_load_dwordx4 v[192:195], %[va], %[ra], %[u] offen offset:1024 sc1\ns_waitcnt vmcnt(0)\n" \
  "v_max_u32 v60, v176, v178\nv_max3_u32 v60, v60, v180, v182\n" \
  "v_max3_u32 v60, v60, v184, v186\nv_max3_u32 v60, v60, v188, v190\n" \
  "v_max3_u32 v60, v60, v192, v194\nv_cmp_eq_u32 vcc, -1, v60\n" \
  "s_cbranch_vccnz 103b\ns_branch 203f\n" \
  "303:\ns_mov_b32 %[n], 0\n" \
  "s_branch 203f\n104:\n" \
  "s_sub_u32 %[n], %[n], 1\ns_cbranch_scc1 304f\n" \
  "s_add_u32 %[u], %[sa], 20480\nbuffer_load_dwordx4 v[196:199], %[va], %[ra], %[u] offen offset:0 sc1\n" \
  "buffer_load_dwordx4 v[200:203], %[va], %[ra], %[u] offen offset:256 sc1\nbuffer_load_dwordx4 v[204:207], %[va], %[ra], %[u] offen offset:512 sc1\n" \
  "buffer_load_dwordx4 v[208:211], %[va], %[ra], %[u] offen offset:768 sc1\nbuffer_load_dwordx4 v[212:215], %[va], %[ra], %[u] offen offset:1024 sc1\n" \
  "s_waitcnt vmcnt(0)\nv_max_u32 v60, v196, v198\n" \
  "v_max3_u32 v60, v60, v200, v202\nv_max3_u32 v60, v60, v204, v206\n" \
  "v_max3_u32 v60, v60, v208, v210\nv_max3_u32 v60, v60, v212, v214\n" \
  "v_cmp_eq_u32 vcc, -1, v60\ns_cbranch_vccnz 104b\n" \
  "s_branch 204f\n304:\n" \
  "s_mov_b32 %[n], 0\ns_branch 204f\n" \
  "105:\ns_sub_u32 %[n], %[n], 1\n" \
  "s_cbranch_scc1 305f\ns_add_u32 %[u], %[sa], 25600\n" \
  "buffer_load_dwordx4 v[216:219], %[va], %[ra], %[u] offen offset:0 sc1\nbuffer_load_dwordx4 v[220:223], %[va], %[ra], %[u] offen offset:256 sc1\n" \
  "buffer_load_dwordx4 v[224:227], %[va], %[ra], %[u] offen offset:512 sc1\nbuffer_load_dwordx4 v[228:231], %[va], %[ra], %[u] offen offset:768 sc1\n" \
  "buffer_load_dwordx4 v[232:235], %[va], %[ra], %[u] offen offset:1024 sc1\ns_waitcnt vmcnt(0)\n" \
  "v_max_u32 v60, v216, v218\nv_max3_u32 v60, v60, v220, v222\n" \
  "v_max3_u32 v60, v60, v224, v226\nv_max3_u32 v60, v60, v228, v230\n" \
  "v_max3_u32 v60, v60, v232, v234\nv_cmp_eq_u32 vcc, -1, v60\n" \
  "s_cbranch_vccnz 105b\ns_branch 205f\n" \
  "305:\ns_mov_b32 %[n], 0\n" \
  "s_branch 205f\n106:\n" \
  "s_sub_u32 %[n], %[n], 1\ns_cbranch_scc1 306f\n" \
  "s_add_u32 %[u], %[sa], 30720\nbuffer_load_dwordx4 v[236:239], %[va], %[ra], %[u] offen offset:0 sc1\n" \
  "buffer_load_dwordx4 v[240:243], %[va], %[ra], %[u] offen offset:256 sc1\nbuffer_load_dwordx4 v[244:247], %[va], %[ra], %[u] offen offset:512 sc1\n" \
  "buffer_load_dwordx4 v[248:251], %[va], %[ra], %[u] offen offset:768 sc1\nbuffer_load_dwordx4 v[252:255], %[va], %[ra], %[u] offen offset:1024 sc1\n" \
  "s_waitcnt vmcnt(0)\nv_max_u32 v60, v236, v238\n" \
  "v_max3_u32 v60, v60, v240, v242\nv_max3_u32 v60, v60, v244, v246\n" \
  "v_max3_u32 v60, v60, v248, v250\nv_max3_u32 v60, v60, v252, v254\n" \
  "v_cmp_eq_u32 vcc, -1, v60\ns_cbranch_vccnz 106b\n" \
  "s_branch 206f\n306:\n" \
  "s_mov_b32 %[n], 0\ns_branch 206f\n" \
  "107:\ns_sub_u32 %[n], %[n], 1\n" \
  "s_cbranch_scc1 307f\ns_add_u32 %[u], %[sa], 35840\n" \
  "buffer_load_dwordx4 v[116:119], %[va], %[ra], %[u] offen offset:0 sc1\nbuffer_load_dwordx4 v[120:123], %[va], %[ra], %[u] offen offset:256 sc1\n" \
  "buffer_load_dwordx4 v[124:127], %[va], %[ra], %[u] offen offset:512 sc1\nbuffer_load_dwordx4 v[128:131], %[va], %[ra], %[u] offen offset:768 sc1\n" \
  "buffer_load_dwordx4 v[132:135], %[va], %[ra], %[u] offen offset:1024 sc1\ns_waitcnt vmcnt(0)\n" \
  "v_max_u32 v60, v116, v118\nv_max3_u32 v60, v60, v120, v122\n" \
  "v_max3_u32 v60, v60, v124, v126\nv_max3_u32 v60, v60, v128, v130\n" \
  "v_max3_u32 v60, v60, v132, v134\nv_cmp_eq_u32 vcc, -1, v60\n" \
  "s_cbranch_vccnz 107b\ns_branch 207f\n" \
  "307:\ns_mov_b32 %[n], 0\n" \
  "s_branch 207f\n108:\n" \
  "s_sub_u32 %[n], %[n], 1\ns_cbranch_scc1 308f\n" \
  "s_add_u32 %[u], %[sa], 40960\nbuffer_load_dwordx4 v[136:139], %[va], %[ra], %[u] offen offset:0 sc1\n" \
  "buffer_load_dwordx4 v[140:143], %[va], %[ra], %[u] offen offset:256 sc1\nbuffer_load_dwordx4 v[144:147], %[va], %[ra], %[u] offen offset:512 sc1\n" \
  "buffer_load_dwordx4 v[148:151], %[va], %[ra], %[u] offen offset:768 sc1\nbuffer_load_dwordx4 v[152:155], %[va], %[ra], %[u] offen offset:1024 sc1\n" \
  "s_waitcnt vmcnt(0)\nv_max_u32 v60, v136, v138\n" \
  "v_max3_u32 v60, v60, v140, v142\nv_max3_u32 v60, v60, v144, v146\n" \
  "v_max3_u32 v60, v60, v148, v150\nv_max3_u32 v60, v60, v152, v154\n" \
  "v_cmp_eq_u32 vcc, -1, v60\ns_cbranch_vccnz 108b\n" \
  "s_branch 208f\n308:\n" \
  "s_mov_b32 %[n], 0\ns_branch 208f\n" \
  "109:\ns_sub_u32 %[n], %[n], 1\n" \
  "s_cbranch_scc1 309f\ns_add_u32 %[u], %[sa], 46080\n" \
  "buffer_load_dwordx4 v[156:159], %[va], %[ra], %[u] offen offset:0 sc1\nbuffer_load_dwordx4 v[160:163], %[va], %[ra], %[u] offen offset:256 sc1\n" \
  "buffer_load_dwordx4 v[164:167], %[va], %[ra], %[u] offen offset:512 sc1\nbuffer_load_dwordx4 v[168:171], %[va], %[ra], %[u] offen offset:768 sc1\n" \
  "buffer_load_dwordx4 v[172:175], %[va], %[ra], %[u] offen offset:1024 sc1\ns_waitcnt vmcnt(0)\n" \
  "v_max_u32 v60, v156, v158\nv_max3_u32 v60, v60, v160, v162\n" \
  "v_max3_u32 v60, v60, v164, v166\nv_max3_u32 v60, v60, v168, v170\n" \
  "v_max3_u32 v60, v60, v172, v174\nv_cmp_eq_u32 vcc, -1, v60\n" \
  "s_cbranch_vccnz 109b\ns_branch 209f\n" \
  "309:\ns_mov_b32 %[n], 0\n" \
  "s_branch 209f\n110:\n" \
  "s_sub_u32 %[n], %[n], 1\ns_cbranch_scc1 310f\n" \
  "s_add_u32 %[u], %[sa], 51200\nbuffer_load_dwordx4 v[176:179], %[va], %[ra], %[u] offen offset:0 sc1\n" \
  "buffer_load_dwordx4 v[180:183], %[va], %[ra], %[u] offen offset:256 sc1\nbuffer_load_dwordx4 v[184:187], %[va], %[ra], %[u] offen offset:512 sc1\n" \
  "buffer_load_dwordx4 v[188:191], %[va], %[ra], %[u] offen offset:768 sc1\nbuffer_load_dwordx4 v[192:195], %[va], %[ra], %[u] offen offset:1024 sc1\n" \
  "s_waitcnt vmcnt(0)\nv_max_u32 v60, v176, v178\n" \
  "v_max3_u32 v60, v60, v180, v182\nv_max3_u32 v60, v60, v184, v186\n" \
  "v_max3_u32 v60, v60, v188, v190\nv_max3_u32 v60, v60, v192, v194\n" \
  "v_cmp_eq_u32 vcc, -1, v60\ns_cbranch_vccnz 110b\n" \
  "s_branch 210f\n310:\n" \
  "s_mov_b32 %[n], 0\ns_branch 210f\n" \
  "111:\ns_sub_u32 %[n], %[n], 1\n" \
  "s_cbranch_scc1 311f\ns_add_u32 %[u], %[sa], 56320\n" \
  "buffer_load_dwordx4 v[196:199], %[va], %[ra], %[u] offen offset:0 sc1\nbuffer_load_dwordx4 v[200:203], %[va], %[ra], %[u] offen offset:256 sc1\n" \
  "buffer_load_dwordx4 v[204:207], %[va], %[ra], %[u] offen offset:512 sc1\nbuffer_load_dwordx4 v[208:211], %[va], %[ra], %[u] offen offset:768 sc1\n" \
  "buffer_load_dwordx4 v[212:215], %[va], %[ra], %[u] offen offset:1024 sc1\ns_waitcnt vmcnt(0)\n" \
  "v_max_u32 v60, v196, v198\nv_max3_u32 v60, v60, v200, v202\n" \
  "v_max3_u32 v60, v60, v204, v206\nv_max3_u32 v60, v60, v208, v210\n" \
  "v_max3_u32 v60, v60, v212, v214\nv_cmp_eq_u32 vcc, -1, v60\n" \
  "s_cbranch_vccnz 111b\ns_branch 211f\n" \
  "311:\ns_mov_b32 %[n], 0\n" \
  "s_branch 211f\n112:\n" \
  "s_sub_u32 %[n], %[n], 1\ns_cbranch_scc1 312f\n" \
  "s_add_u32 %[u], %[sa], 61440\nbuffer_load_dwordx4 v[216:219], %[va], %[ra], %[u] offen offset:0 sc1\n" \
  "buffer_load_dwordx4 v[220:223], %[va], %[ra], %[u] offen offset:256 sc1\nbuffer_load_dwordx4 v[224:227], %[va], %[ra], %[u] offen offset:512 sc1\n" \
  "buffer_load_dwordx4 v[228:231], %[va], %[ra], %[u] offen offset:768 sc1\nbuffer_load_dwordx4 v[232:235], %[va], %[ra], %[u] offen offset:1024 sc1\n" \
  "s_waitcnt vmcnt(0)\nv_max_u32 v60, v216, v218\n" \
  "v_max3_u32 v60, v60, v220, v222\nv_max3_u32 v60, v60, v224, v226\n" \
  "v_max3_u32 v60, v60, v228, v230\nv_max3_u32 v60, v60, v232, v234\n" \
  "v_cmp_eq_u32 vcc, -1, v60\ns_cbranch_vccnz 112b\n" \
  "s_branch 212f\n312:\n" \
  "s_mov_b32 %[n], 0\ns_branch 212f\n" \
  "113:\ns_sub_u32 %[n], %[n], 1\n" \
  "s_cbranch_scc1 313f\ns_add_u32 %[u], %[sa], 66560\n" \
  "buffer_load_dwordx4 v[236:239], %[va], %[ra], %[u] offen offset:0 sc1\nbuffer_load_dwordx4 v[240:243], %[va], %[ra], %[u] offen offset:256 sc1\n" \
  "buffer_load_dwordx4 v[244:247], %[va], %[ra], %[u] offen offset:512 sc1\nbuffer_load_dwordx4 v[248:251], %[va], %[ra], %[u] offen offset:768 sc1\n" \
  "buffer_load_dwordx4 v[252:255], %[va], %[ra], %[u] offen offset:1024 sc1\ns_waitcnt vmcnt(0)\n" \
  "v_max_u32 v60, v236, v238\nv_max3_u32 v60, v60, v240, v242\n" \
  "v_max3_u32 v60, v60, v244, v246\nv_max3_u32 v60, v60, v248, v250\n" \
  "v_max3_u32 v60, v60, v252, v254\nv_cmp_eq_u32 vcc, -1, v60\n" \
  "s_cbranch_vccnz 113b\ns_branch 213f\n" \
  "313:\ns_mov_b32 %[n], 0\n" \
  "s_branch 213f\n3:\n" \
  "buffer_load_dword v61, %[vp], %[ra], %[sa] offen offset:1276 sc1\ns_waitcnt vmcnt(0)\n" \
  "v_cmp_eq_u32 vcc, -1, v61\ns_cbranch_vccz 4f\n" \
  "s_sub_u32 %[n], %[n], 1\ns_cbranch_scc0 3b\n" \
  "s_mov_b32 %[n], 0\n4:\n" \
  "s_mov_b32 %[k], %[sa]\nds_read_b64_tr_b16 v[84:85], v56 offset:0\n" \
  "ds_read_b64_tr_b16 v[86:87], v56 offset:512\nds_read_b64_tr_b16 v[88:89], v57 offset:0\n" \
  "ds_read_b64_tr_b16 v[90:91], v57 offset:512\nds_read_b64_tr_b16 v[92:93], v58 offset:0\n" \
  "ds_read_b64_tr_b16 v[94:95], v58 offset:512\nds_read_b64_tr_b16 v[96:97], v59 offset:0\n" \
  "ds_read_b64_tr_b16 v[98:99], v59 offset:512\nbuffer_load_dwordx4 v[116:119], %[va], %[ra], %[k] offen offset:0 sc1\n" \
  "buffer_load_dwordx4 v[120:123], %[va], %[ra], %[k] offen offset:256 sc1\nbuffer_load_dwordx4 v[124:127], %[va], %[ra], %[k] offen offset:512 sc1\n" \
  "buffer_load_dwordx4 v[128:131], %[va], %[ra], %[k] offen offset:768 sc1\nbuffer_load_dwordx4 v[132:135], %[va], %[ra], %[k] offen offset:1024 sc1\n" \
  "s_add_u32 %[k], %[k], 5120\nbuffer_load_dwordx4 v[136:139], %[va], %[ra], %[k] offen offset:0 sc1\n" \
  "buffer_load_dwordx4 v[140:143], %[va], %[ra], %[k] offen offset:256 sc1\nbuffer_load_dwordx4 v[144:147], %[va], %[ra], %[k] offen offset:512 sc1\n" \
  "buffer_load_dwordx4 v[148:151], %[va], %[ra], %[k] offen offset:768 sc1\nbuffer_load_dwordx4 v[152:155], %[va], %[ra], %[k] offen offset:1024 sc1\n" \
  "s_add_u32 %[k], %[k], 5120\nbuffer_load_dwordx4 v[156:159], %[va], %[ra], %[k] offen offset:0 sc1\n" \
  "buffer_load_dwordx4 v[160:163], %[va], %[ra], %[k] offen offset:256 sc1\nbuffer_load_dwordx4 v[164:167], %[va], %[ra], %[k] offen offset:512 sc1\n" \
  "buffer_load_dwordx4 v[168:171], %[va], %[ra], %[k] offen offset:768 sc1\nbuffer_load_dwordx4 v[172:175], %[va], %[ra], %[k] offen offset:1024 sc1\n" \
  "s_add_u32 %[k], %[k], 5120\nbuffer_load_dwordx4 v[176:179], %[va], %[ra], %[k] offen offset:0 sc1\n" \
  "buffer_load_dwordx4 v[180:183], %[va], %[ra], %[k] offen offset:256 sc1\nbuffer_load_dwordx4 v[184:187], %[va], %[ra], %[k] offen offset:512 sc1\n" \
  "buffer_load_dwordx4 v[188:191], %[va], %[ra], %[k] offen offset:768 sc1\nbuffer_load_dwordx4 v[192:195], %[va], %[ra], %[k] offen offset:1024 sc1\n" \
  "s_add_u32 %[k], %[k], 5120\ns_xor_b32 %[u], %[wv], 1\n" \
  "s_mul_i32 %[u], %[u], 40960\nv_add_u32 v61, %[u], %[vl]\n" \
  "s_xor_b32 %[u], %[wv], 2\ns_mul_i32 %[u], %[u], 40960\n" \
  "v_add_u32 v62, %[u], %[vl]\ns_xor_b32 %[u], %[wv], 3\n" \
  "s_mul_i32 %[u], %[u], 40960\nv_add_u32 v63, %[u], %[vl]\n" \
  "s_waitcnt vmcnt(15)\nv_max_u32 v60, v116, v118\n" \
  "v_max3_u32 v60, v60, v120, v122\nv_max3_u32 v60, v60, v124, v126\n" \
  "v_max3_u32 v60, v60, v128, v130\nv_max3_u32 v60, v60, v132, v134\n" \
  "v_cmp_eq_u32 vcc, -1, v60\ns_cbranch_vccnz 100b\n" \
  "200:\ns_waitcnt lgkmcnt(0)\n" \
  "v_mfma_f32_16x16x32_bf16 v[64:67], v[116:119], v[84:87], 0\nbuffer_load_dwordx4 v[196:199], %[va], %[ra], %[k] offen offset:0 sc1\n" \
  "v_mfma_f32_16x16x32_bf16 v[68:71], v[120:123], v[84:87], 0\nds_read_b64_tr_b16 v[100:101], v56 offset:4352\n" \
  "v_mfma_f32_16x16x32_bf16 v[72:75], v[124:127], v[84:87], 0\nds_read_b64_tr_b16 v[102:103], v56 offset:4864\n" \
  "v_mfma_f32_16x16x32_bf16 v[76:79], v[128:131], v[84:87], 0\nds_read_b64_tr_b16 v[104:105], v57 offset:4352\n" \
  "v_mfma_f32_16x16x32_bf16 v[80:83], v[132:135], v[84:87], 0\nbuffer_load_dwordx4 v[200:203], %[va], %[ra], %[k] offen offset:256 sc1\n" \
  "v_mfma_f32_16x16x32_bf16 a[192:195], v[116:119], v[88:91], 0\nds_read_b64_tr_b16 v[106:107], v57 offset:4864\n" \
  "v_mfma_f32_16x16x32_bf16 a[196:199], v[120:123], v[88:91], 0\nds_read_b64_tr_b16 v[108:109], v58 offset:4352\n" \
  "v_mfma_f32_16x16x32_bf16 a[200:203], v[124:127], v[88:91], 0\nds_read_b64_tr_b16 v[110:111], v58 offset:4864\n" \
  "v_mfma_f32_16x16x32_bf16 a[204:207], v[128:131], v[88:91], 0\nbuffer_load_dwordx4 v[204:207], %[va], %[ra], %[k] offen offset:512 sc1\n" \
  "v_mfma_f32_16x16x32_bf16 a[208:211], v[132:135], v[88:91], 0\nds_read_b64_tr_b16 v[112:113], v59 offset:4352\n" \
  "v_mfma_f32_16x16x32_bf16 a[212:215], v[116:119], v[92:95], 0\nds_read_b64_tr_b16 v[114:115], v59 offset:4864\n" \
  "v_mfma_f32_16x16x32_bf16 a[216:219], v[120:123], v[92:95], 0\ns_waitcnt vmcnt(13)\n" \
  "v_max_u32 v60, v136, v138\nv_mfma_f32_16x16x32_bf16 a[220:223], v[124:127], v[92:95], 0\n" \
  "buffer_load_dwordx4 v[208:211], %[va], %[ra], %[k] offen offset:768 sc1\nv_mfma_f32_16x16x32_bf16 a[224:227], v[128:131], v[92:95], 0\n" \
  "v_max3_u32 v60, v60, v140, v142\nv_mfma_f32_16x16x32_bf16 a[228:231], v[132:135], v[92:95], 0\n" \
  "v_max3_u32 v60, v60, v144, v146\nv_mfma_f32_16x16x32_bf16 a[232:235], v[116:119], v[96:99], 0\n" \
  "v_max3_u32 v60, v60, v148, v150\nv_mfma_f32_16x16x32_bf16 a[236:239], v[120:123], v[96:99], 0\n" \
  "buffer_load_dwordx4 v[212:215], %[va], %[ra], %[k] offen offset:1024 sc1\ns_add_u32 %[k], %[k], 5120\n" \
  "v_mfma_f32_16x16x32_bf16 a[240:243], v[124:127], v[96:99], 0\nv_max3_u32 v60, v60, v152, v154\n" \
  "v_mfma_f32_16x16x32_bf16 a[244:247], v[128:131], v[96:99], 0\nv_cmp_eq_u32 vcc, -1, v60\n" \
  "v_mfma_f32_16x16x32_bf16 a[248:251], v[132:135], v[96:99], 0\ns_cbranch_vccnz 101b\n" \
  "201:\ns_waitcnt lgkmcnt(0)\n" \
  "v_mfma_f32_16x16x32_bf16 v[64:67], v[136:139], v[100:103], v[64:67]\nbuffer_load_dwordx4 v[216:219], %[va], %[ra], %[k] offen offset:0 sc1\n" \
  "v_mfma_f32_16x16x32_bf16 v[68:71], v[140:143], v[100:103], v[68:71]\nds_read_b64_tr_b16 v[84:85], v56 offset:8704\n" \
  "v_mfma_f32_16x16x32_bf16 v[72:75], v[144:147], v[100:103], v[72:75]\nds_read_b64_tr_b16 v[86:87], v56 offset:9216\n" \
  "v_mfma_f32_16x16x32_bf16 v[76:79], v[148:151], v[100:103], v[76:79]\nds_read_b64_tr_b16 v[88:89], v57 offset:8704\n" \
  "v_mfma_f32_16x16x32_bf16 v[80:83], v[152:155], v[100:103], v[80:83]\nbuffer_load_dwordx4 v[220:223], %[va], %[ra], %[k] offen offset:256 sc1\n" \
  "v_mfma_f32_16x16x32_bf16 a[192:195], v[136:139], v[104:107], a[192:195]\nds_read_b64_tr_b16 v[90:91], v57 offset:9216\n" \
  "v_mfma_f32_16x16x32_bf16 a[196:199], v[140:143], v[104:107], a[196:199]\nds_read_b64_tr_b16 v[92:93], v58 offset:8704\n" \
  "v_mfma_f32_16x16x32_bf16 a[200:203], v[144:147], v[104:107], a[200:203]\nds_read_b64_tr_b16 v[94:95], v58 offset:9216\n" \
  "v_mfma_f32_16x16x32_bf16 a[204:207], v[148:151], v[104:107], a[204:207]\nbuffer_load_dwordx4 v[224:227], %[va], %[ra], %[k] offen offset:512 sc1\n" \
  "v_mfma_f32_16x16x32_bf16 a[208:211], v[152:155], v[104:107], a[208:211]\nds_read_b64_tr_b16 v[96:97], v59 offset:8704\n" \
  "v_mfma_f32_16x16x32_bf16 a[212:215], v[136:139], v[108:111], a[212:215]\nds_read_b64_tr_b16 v[98:99], v59 offset:9216\n" \
  "v_mfma_f32_16x16x32_bf16 a[216:219], v[140:143], v[108:111], a[216:219]\ns_waitcnt vmcnt(13)\n" \
  "v_max_u32 v60, v156, v158\nv_mfma_f32_16x16x32_bf16 a[220:223], v[144:147], v[108:111], a[220:223]\n" \
  "buffer_load_dwordx4 v[228:231], %[va], %[ra], %[k] offen offset:768 sc1\nv_mfma_f32_16x16x32_bf16 a[224:227], v[148:151], v[108:111], a[224:227]\n" \
  "v_max3_u32 v60, v60, v160, v162\nv_mfma_f32_16x16x32_bf16 a[228:231], v[152:155], v[108:111], a[228:231]\n" \
  "v_max3_u32 v60, v60, v164, v166\nv_mfma_f32_16x16x32_bf16 a[232:235], v[136:139], v[112:115], a[232:235]\n" \
  "v_max3_u32 v60, v60, v168, v170\nv_mfma_f32_16x16x32_bf16 a[236:239], v[140:143], v[112:115], a[236:239]\n" \
  "buffer_load_dwordx4 v[232:235], %[va], %[ra], %[k] offen offset:1024 sc1\ns_add_u32 %[k], %[k], 5120\n" \
  "v_mfma_f32_16x16x32_bf16 a[240:243], v[144:147], v[112:115], a[240:243]\nv_max3_u32 v60, v60, v172, v174\n" \
  "v_mfma_f32_16x16x32_bf16 a[244:247], v[148:151], v[112:115], a[244:247]\nv_cmp_eq_u32 vcc, -1, v60\n" \
  "v_mfma_f32_16x16x32_bf16 a[248:251], v[152:155], v[112:115], a[248:251]\ns_cbranch_vccnz 102b\n" \
  "202:\ns_waitcnt lgkmcnt(0)\n" \
  "v_mfma_f32_16x16x32_bf16 v[64:67], v[156:159], v[84:87], v[64:67]\nbuffer_load_dwordx4 v[236:239], %[va], %[ra], %[k] offen offset:0 sc1\n" \
  "v_mfma_f32_16x16x32_bf16 v[68:71], v[160:163], v[84:87], v[68:71]\nds_read_b64_tr_b16 v[100:101], v56 offset:13056\n" \
  "v_mfma_f32_16x16x32_bf16 v[72:75], v[164:167], v[84:87], v[72:75]\nds_read_b64_tr_b16 v[102:103], v56 offset:13568\n" \
  "v_mfma_f32_16x16x32_bf16 v[76:79], v[168:171], v[84:87], v[76:79]\nds_read_b64_tr_b16 v[104:105], v57 offset:13056\n" \
  "v_mfma_f32_16x16x32_bf16 v[80:83], v[172:175], v[84:87], v[80:83]\nbuffer_load_dwordx4 v[240:243], %[va], %[ra], %[k] offen offset:256 sc1\n" \
  "v_mfma_f32_16x16x32_bf16 a[192:195], v[156:159], v[88:91], a[192:195]\nds_read_b64_tr_b16 v[106:107], v57 offset:13568\n" \
  "v_mfma_f32_16x16x32_bf16 a[196:199], v[160:163], v[88:91], a[196:199]\nds_read_b64_tr_b16 v[108:109], v58 offset:13056\n" \
  "v_mfma_f32_16x16x32_bf16 a[200:203], v[164:167], v[88:91], a[200:203]\nds_read_b64_tr_b16 v[110:111], v58 offset:13568\n" \
  "v_mfma_f32_16x16x32_bf16 a[204:207], v[168:171], v[88:91], a[204:207]\nbuffer_load_dwordx4 v[244:247], %[va], %[ra], %[k] offen offset:512 sc1\n" \
  "v_mfma_f32_16x16x32_bf16 a[208:211], v[172:175], v[88:91], a[208:211]\nds_read_b64_tr_b16 v[112:113], v59 offset:13056\n" \
  "v_mfma_f32_16x16x32_bf16 a[212:215], v[156:159], v[92:95], a[212:215]\nds_read_b64_tr_b16 v[114:115], v59 offset:13568\n" \
  "v_mfma_f32_16x16x32_bf16 a[216:219], v[160:163], v[92:95], a[216:219]\ns_waitcnt vmcnt(13)\n" \
  "v_max_u32 v60, v176, v178\nv_mfma_f32_16x16x32_bf16 a[220:223], v[164:167], v[92:95], a[220:223]\n" \
  "buffer_load_dwordx4 v[248:251], %[va], %[ra], %[k] offen offset:768 sc1\nv_mfma_f32_16x16x32_bf16 a[224:227], v[168:171], v[92:95], a[224:227]\n" \
  "v_max3_u32 v60, v60, v180, v182\nv_mfma_f32_16x16x32_bf16 a[228:231], v[172:175], v[92:95], a[228:231]\n" \
  "v_max3_u32 v60, v60, v184, v186\nv_mfma_f32_16x16x32_bf16 a[232:235], v[156:159], v[96:99], a[232:235]\n" \
  "v_max3_u32 v60, v60, v188, v190\nv_mfma_f32_16x16x32_bf16 a[236:239], v[160:163], v[96:99], a[236:239]\n" \
  "buffer_load_dwordx4 v[252:255], %[va], %[ra], %[k] offen offset:1024 sc1\ns_add_u32 %[k], %[k], 5120\n" \
  "v_mfma_f32_16x16x32_bf16 a[240:243], v[164:167], v[96:99], a[240:243]\nv_max3_u32 v60, v60, v192, v194\n" \
  "v_mfma_f32_16x16x32_bf16 a[244:247], v[168:171], v[96:99], a[244:247]\nv_cmp_eq_u32 vcc, -1, v60\n" \
  "v_mfma_f32_16x16x32_bf16 a[248:251], v[172:175], v[96:99], a[248:251]\ns_cbranch_vccnz 103b\n" \
  "203:\ns_waitcnt lgkmcnt(0)\n" \
  "v_mfma_f32_16x16x32_bf16 v[64:67], v[176:179], v[100:103], v[64:67]\nbuffer_load_dwordx4 v[116:119], %[va], %[ra], %[k] offen offset:0 sc1\n" \
  "v_mfma_f32_16x16x32_bf16 v[68:71], v[180:183], v[100:103], v[68:71]\nds_read_b64_tr_b16 v[84:85], v56 offset:34816\n" \
  "v_mfma_f32_16x16x32_bf16 v[72:75], v[184:187], v[100:103], v[72:75]\nds_read_b64_tr_b16 v[86:87], v56 offset:35328\n" \
  "v_mfma_f32_16x16x32_bf16 v[76:79], v[188:191], v[100:103], v[76:79]\nds_read_b64_tr_b16 v[88:89], v57 offset:34816\n" \
  "v_mfma_f32_16x16x32_bf16 v[80:83], v[192:195], v[100:103], v[80:83]\nbuffer_load_dwordx4 v[120:123], %[va], %[ra], %[k] offen offset:256 sc1\n" \
  "v_mfma_f32_16x16x32_bf16 a[192:195], v[176:179], v[104:107], a[192:195]\nds_read_b64_tr_b16 v[90:91], v57 offset:35328\n" \
  "v_mfma_f32_16x16x32_bf16 a[196:199], v[180:183], v[104:107], a[196:199]\nds_read_b64_tr_b16 v[92:93], v58 offset:34816\n" \
  "v_mfma_f32_16x16x32_bf16 a[200:203], v[184:187], v[104:107], a[200:203]\nds_read_b64_tr_b16 v[94:95], v58 offset:35328\n" \
  "v_mfma_f32_16x16x32_bf16 a[204:207], v[188:191], v[104:107], a[204:207]\nbuffer_load_dwordx4 v[124:127], %[va], %[ra], %[k] offen offset:512 sc1\n" \
  "v_mfma_f32_16x16x32_bf16 a[208:211], v[192:195], v[104:107], a[208:211]\nds_read_b64_tr_b16 v[96:97], v59 offset:34816\n" \
  "v_mfma_f32_16x16x32_bf16 a[212:215], v[176:179], v[108:111], a[212:215]\nds_read_b64_tr_b16 v[98:99], v59 offset:35328\n" \
  "v_mfma_f32_16x16x32_bf16 a[216:219], v[180:183], v[108:111], a[216:219]\ns_waitcnt vmcnt(13)\n" \
  "v_max_u32 v60, v196, v198\nv_mfma_f32_16x16x32_bf16 a[220:223], v[184:187], v[108:111], a[220:223]\n" \
  "buffer_load_dwordx4 v[128:131], %[va], %[ra], %[k] offen offset:768 sc1\nv_mfma_f32_16x16x32_bf16 a[224:227], v[188:191], v[108:111], a[224:227]\n" \
  "v_max3_u32 v60, v60, v200, v202\nv_mfma_f32_16x16x32_bf16 a[228:231], v[192:195], v[108:111], a[228:231]\n" \
  "v_max3_u32 v60, v60, v204, v206\nv_mfma_f32_16x16x32_bf16 a[232:235], v[176:179], v[112:115], a[232:235]\n" \
  "v_max3_u32 v60, v60, v208, v210\nv_mfma_f32_16x16x32_bf16 a[236:239], v[180:183], v[112:115], a[236:239]\n" \
  "buffer_load_dwordx4 v[132:135], %[va], %[ra], %[k] offen offset:1024 sc1\ns_add_u32 %[k], %[k], 5120\n" \
  "v_mfma_f32_16x16x32_bf16 a[240:243], v[184:187], v[112:115], a[240:243]\nv_max3_u32 v60, v60, v212, v214\n" \
  "v_mfma_f32_16x16x32_bf16 a[244:247], v[188:191], v[112:115], a[244:247]\nv_cmp_eq_u32 vcc, -1, v60\n" \
  "v_mfma_f32_16x16x32_bf16 a[248:251], v[192:195], v[112:115], a[248:251]\ns_cbranch_vccnz 104b\n" \
  "204:\ns_waitcnt lgkmcnt(0)\n" \
  "v_mfma_f32_16x16x32_bf16 v[64:67], v[196:199], v[84:87], v[64:67]\nbuffer_load_dwordx4 v[136:139], %[va], %[ra], %[k] offen offset:0 sc1\n" \
  "v_mfma_f32_16x16x32_bf16 v[68:71], v[200:203], v[84:87], v[68:71]\nds_read_b64_tr_b16 v[100:101], v56 offset:17408\n" \
  "v_mfma_f32_16x16x32_bf16 v[72:75], v[204:207], v[84:87], v[72:75]\nds_read_b64_tr_b16 v[102:103], v56 offset:17920\n" \
  "v_mfma_f32_16x16x32_bf16 v[76:79], v[208:211], v[84:87], v[76:79]\nds_read_b64_tr_b16 v[104:105], v57 offset:17408\n" \
  "v_mfma_f32_16x16x32_bf16 v[80:83], v[212:215], v[84:87], v[80:83]\nbuffer_load_dwordx4 v[140:143], %[va], %[ra], %[k] offen offset:256 sc1\n" \
  "v_mfma_f32_16x16x32_bf16 a[192:195], v[196:199], v[88:91], a[192:195]\nds_read_b64_tr_b16 v[106:107], v57 offset:17920\n" \
  "v_mfma_f32_16x16x32_bf16 a[196:199], v[200:203], v[88:91], a[196:199]\nds_read_b64_tr_b16 v[108:109], v58 offset:17408\n" \
  "v_mfma_f32_16x16x32_bf16 a[200:203], v[204:207], v[88:91], a[200:203]\nds_read_b64_tr_b16 v[110:111], v58 offset:17920\n" \
  "v_mfma_f32_16x16x32_bf16 a[204:207], v[208:211], v[88:91], a[204:207]\nbuffer_load_dwordx4 v[144:147], %[va], %[ra], %[k] offen offset:512 sc1\n" \
  "v_mfma_f32_16x16x32_bf16 a[208:211], v[212:215], v[88:91], a[208:211]\nds_read_b64_tr_b16 v[112:113], v59 offset:17408\n" \
  "v_mfma_f32_16x16x32_bf16 a[212:215], v[196:199], v[92:95], a[212:215]\nds_read_b64_tr_b16 v[114:115], v59 offset:17920\n" \
  "v_mfma_f32_16x16x32_bf16 a[216:219], v[200:203], v[92:95], a[216:219]\ns_waitcnt vmcnt(13)\n" \
  "v_max_u32 v60, v216, v218\nv_mfma_f32_16x16x32_bf16 a[220:223], v[204:207], v[92:95], a[220:223]\n" \
  "buffer_load_dwordx4 v[148:151], %[va], %[ra], %[k] offen offset:768 sc1\nv_mfma_f32_16x16x32_bf16 a[224:227], v[208:211], v[92:95], a[224:227]\n" \
  "v_max3_u32 v60, v60, v220, v222\nv_mfma_f32_16x16x32_bf16 a[228:231], v[212:215], v[92:95], a[228:231]\n" \
  "v_max3_u32 v60, v60, v224, v226\nv_mfma_f32_16x16x32_bf16 a[232:235], v[196:199], v[96:99], a[232:235]\n" \
  "v_max3_u32 v60, v60, v228, v230\nv_mfma_f32_16x16x32_bf16 a[236:239], v[200:203], v[96:99], a[236:239]\n" \
  "buffer_load_dwordx4 v[152:155], %[va], %[ra], %[k] offen offset:1024 sc1\ns_add_u32 %[k], %[k], 5120\n" \
  "v_mfma_f32_16x16x32_bf16 a[240:243], v[204:207], v[96:99], a[240:243]\nv_max3_u32 v60, v60, v232, v234\n" \
  "v_mfma_f32_16x16x32_bf16 a[244:247], v[208:211], v[96:99], a[244:247]\nv_cmp_eq_u32 vcc, -1, v60\n" \
  "v_mfma_f32_16x16x32_bf16 a[248:251], v[212:215], v[96:99], a[248:251]\ns_cbranch_vccnz 105b\n" \
  "205:\ns_waitcnt lgkmcnt(0)\n" \
  "v_mfma_f32_16x16x32_bf16 v[64:67], v[216:219], v[100:103], v[64:67]\nbuffer_load_dwordx4 v[156:159], %[va], %[ra], %[k] offen offset:0 sc1\n" \
  "v_mfma_f32_16x16x32_bf16 v[68:71], v[220:223], v[100:103], v[68:71]\nds_read_b64_tr_b16 v[84:85], v56 offset:21760\n" \
  "v_mfma_f32_16x16x32_bf16 v[72:75], v[224:227], v[100:103], v[72:75]\nds_read_b64_tr_b16 v[86:87], v56 offset:22272\n" \
  "v_mfma_f32_16x16x32_bf16 v[76:79], v[228:231], v[100:103], v[76:79]\nds_read_b64_tr_b16 v[88:89], v57 offset:21760\n" \
  "v_mfma_f32_16x16x32_bf16 v[80:83], v[232:235], v[100:103], v[80:83]\nbuffer_load_dwordx4 v[160:163], %[va], %[ra], %[k] offen offset:256 sc1\n" \
  "v_mfma_f32_16x16x32_bf16 a[192:195], v[216:219], v[104:107], a[192:195]\nds_read_b64_tr_b16 v[90:91], v57 offset:22272\n" \
  "v_mfma_f32_16x16x32_bf16 a[196:199], v[220:223], v[104:107], a[196:199]\nds_read_b64_tr_b16 v[92:93], v58 offset:21760\n" \
  "v_mfma_f32_16x16x32_bf16 a[200:203], v[224:227], v[104:107], a[200:203]\nds_read_b64_tr_b16 v[94:95], v58 offset:22272\n" \
  "v_mfma_f32_16x16x32_bf16 a[204:207], v[228:231], v[104:107], a[204:207]\nbuffer_load_dwordx4 v[164:167], %[va], %[ra], %[k] offen offset:512 sc1\n" \
  "v_mfma_f32_16x16x32_bf16 a[208:211], v[232:235], v[104:107], a[208:211]\nds_read_b64_tr_b16 v[96:97], v59 offset:21760\n" \
  "v_mfma_f32_16x16x32_bf16 a[212:215], v[216:219], v[108:111], a[212:215]\nds_read_b64_tr_b16 v[98:99], v59 offset:22272\n" \
  "v_mfma_f32_16x16x32_bf16 a[216:219], v[220:223], v[108:111], a[216:219]\ns_waitcnt vmcnt(13)\n" \
  "v_max_u32 v60, v236, v238\nv_mfma_f32_16x16x32_bf16 a[220:223], v[224:227], v[108:111], a[220:223]\n" \
  "buffer_load_dwordx4 v[168:171], %[va], %[ra], %[k] offen offset:768 sc1\nv_mfma_f32_16x16x32_bf16 a[224:227], v[228:231], v[108:111], a[224:227]\n" \
  "v_max3_u32 v60, v60, v240, v242\nv_mfma_f32_16x16x32_bf16 a[228:231], v[232:235], v[108:111], a[228:231]\n" \
  "v_max3_u32 v60, v60, v244, v246\nv_mfma_f32_16x16x32_bf16 a[232:235], v[216:219], v[112:115], a[232:235]\n" \
  "v_max3_u32 v60, v60, v248, v250\nv_mfma_f32_16x16x32_bf16 a[236:239], v[220:223], v[112:115], a[236:239]\n" \
  "buffer_load_dwordx4 v[172:175], %[va], %[ra], %[k] offen offset:1024 sc1\ns_add_u32 %[k], %[k], 5120\n" \
  "v_mfma_f32_16x16x32_bf16 a[240:243], v[224:227], v[112:115], a[240:243]\nv_max3_u32 v60, v60, v252, v254\n" \
  "v_mfma_f32_16x16x32_bf16 a[244:247], v[228:231], v[112:115], a[244:247]\nv_cmp_eq_u32 vcc, -1, v60\n" \
  "v_mfma_f32_16x16x32_bf16 a[248:251], v[232:235], v[112:115], a[248:251]\ns_cbranch_vccnz 106b\n" \
  "206:\ns_waitcnt lgkmcnt(0)\n" \
  "v_mfma_f32_16x16x32_bf16 v[64:67], v[236:239], v[84:87], v[64:67]\nbuffer_load_dwordx4 v[176:179], %[va], %[ra], %[k] offen offset:0 sc1\n" \
  "v_mfma_f32_16x16x32_bf16 v[68:71], v[240:243], v[84:87], v[68:71]\nds_read_b64_tr_b16 v[100:101], v56 offset:26112\n" \
  "v_mfma_f32_16x16x32_bf16 v[72:75], v[244:247], v[84:87], v[72:75]\nds_read_b64_tr_b16 v[102:103], v56 offset:26624\n" \
  "v_mfma_f32_16x16x32_bf16 v[76:79], v[248:251], v[84:87], v[76:79]\nds_read_b64_tr_b16 v[104:105], v57 offset:26112\n" \
  "v_mfma_f32_16x16x32_bf16 v[80:83], v[252:255], v[84:87], v[80:83]\nbuffer_load_dwordx4 v[180:183], %[va], %[ra], %[k] offen offset:256 sc1\n" \
  "v_mfma_f32_16x16x32_bf16 a[192:195], v[236:239], v[88:91], a[192:195]\nds_read_b64_tr_b16 v[106:107], v57 offset:26624\n" \
  "v_mfma_f32_16x16x32_bf16 a[196:199], v[240:243], v[88:91], a[196:199]\nds_read_b64_tr_b16 v[108:109], v58 offset:26112\n" \
  "v_mfma_f32_16x16x32_bf16 a[200:203], v[244:247], v[88:91], a[200:203]\nds_read_b64_tr_b16 v[110:111], v58 offset:26624\n" \
  "v_mfma_f32_16x16x32_bf16 a[204:207], v[248:251], v[88:91], a[204:207]\nbuffer_load_dwordx4 v[184:187], %[va], %[ra], %[k] offen offset:512 sc1\n" \
  "v_mfma_f32_16x16x32_bf16 a[208:211], v[252:255], v[88:91], a[208:211]\nds_read_b64_tr_b16 v[112:113], v59 offset:26112\n" \
  "v_mfma_f32_16x16x32_bf16 a[212:215], v[236:239], v[92:95], a[212:215]\nds_read_b64_tr_b16 v[114:115], v59 offset:26624\n" \
  "v_mfma_f32_16x16x32_bf16 a[216:219], v[240:243], v[92:95], a[216:219]\ns_waitcnt vmcnt(13)\n" \
  "v_max_u32 v60, v116, v118\nv_mfma_f32_16x16x32_bf16 a[220:223], v[244:247], v[92:95], a[220:223]\n" \
  "buffer_load_dwordx4 v[188:191], %[va], %[ra], %[k] offen offset:768 sc1\nv_mfma_f32_16x16x32_bf16 a[224:227], v[248:251], v[92:95], a[224:227]\n" \
  "v_max3_u32 v60, v60, v120, v122\nv_mfma_f32_16x16x32_bf16 a[228:231], v[252:255], v[92:95], a[228:231]\n" \
  "v_max3_u32 v60, v60, v124, v126\nv_mfma_f32_16x16x32_bf16 a[232:235], v[236:239], v[96:99], a[232:235]\n" \
  "v_max3_u32 v60, v60, v128, v130\nv_mfma_f32_16x16x32_bf16 a[236:239], v[240:243], v[96:99], a[236:239]\n" \
  "buffer_load_dwordx4 v[192:195], %[va], %[ra], %[k] offen offset:1024 sc1\ns_add_u32 %[k], %[k], 5120\n" \
  "v_mfma_f32_16x16x32_bf16 a[240:243], v[244:247], v[96:99], a[240:243]\nv_max3_u32 v60, v60, v132, v134\n" \
  "v_mfma_f32_16x16x32_bf16 a[244:247], v[248:251], v[96:99], a[244:247]\nv_cmp_eq_u32 vcc, -1, v60\n" \
  "v_mfma_f32_16x16x32_bf16 a[248:251], v[252:255], v[96:99], a[248:251]\ns_cbranch_vccnz 107b\n" \
  "207:\ns_waitcnt lgkmcnt(0)\n" \
  "v_mfma_f32_16x16x32_bf16 v[64:67], v[116:119], v[100:103], v[64:67]\nbuffer_load_dwordx4 v[196:199], %[va], %[ra], %[k] offen offset:0 sc1\n" \
  "v_mfma_f32_16x16x32_bf16 v[68:71], v[120:123], v[100:103], v[68:71]\nds_read_b64_tr_b16 v[84:85], v56 offset:30464\n" \
  "v_mfma_f32_16x16x32_bf16 v[72:75], v[124:127], v[100:103], v[72:75]\nds_read_b64_tr_b16 v[86:87], v56 offset:30976\n" \
  "v_mfma_f32_16x16x32_bf16 v[76:79], v[128:131], v[100:103], v[76:79]\nds_read_b64_tr_b16 v[88:89], v57 offset:30464\n" \
  "v_mfma_f32_16x16x32_bf16 v[80:83], v[132:135], v[100:103], v[80:83]\nbuffer_load_dwordx4 v[200:203], %[va], %[ra], %[k] offen offset:256 sc1\n" \
  "v_mfma_f32_16x16x32_bf16 a[192:195], v[116:119], v[104:107], a[192:195]\nds_read_b64_tr_b16 v[90:91], v57 offset:30976\n" \
  "v_mfma_f32_16x16x32_bf16 a[196:199], v[120:123], v[104:107], a[196:199]\nds_read_b64_tr_b16 v[92:93], v58 offset:30464\n" \
  "v_mfma_f32_16x16x32_bf16 a[200:203], v[124:127], v[104:107], a[200:203]\nds_read_b64_tr_b16 v[94:95], v58 offset:30976\n" \
  "v_mfma_f32_16x16x32_bf16 a[204:207], v[128:131], v[104:107], a[204:207]\nbuffer_load_dwordx4 v[204:207], %[va], %[ra], %[k] offen offset:512 sc1\n" \
  "v_mfma_f32_16x16x32_bf16 a[208:211], v[132:135], v[104:107], a[208:211]\nds_read_b64_tr_b16 v[96:97], v59 offset:30464\n" \
  "v_mfma_f32_16x16x32_bf16 a[212:215], v[116:119], v[108:111], a[212:215]\nds_read_b64_tr_b16 v[98:99], v59 offset:30976\n" \
  "v_mfma_f32_16x16x32_bf16 a[216:219], v[120:123], v[108:111], a[216:219]\ns_waitcnt vmcnt(13)\n" \
  "v_max_u32 v60, v136, v138\nv_mfma_f32_16x16x32_bf16 a[220:223], v[124:127], v[108:111], a[220:223]\n" \
  "buffer_load_dwordx4 v[208:211], %[va], %[ra], %[k] offen offset:768 sc1\nv_mfma_f32_16x16x32_bf16 a[224:227], v[128:131], v[108:111], a[224:227]\n" \
  "v_max3_u32 v60, v60, v140, v142\nv_mfma_f32_16x16x32_bf16 a[228:231], v[132:135], v[108:111], a[228:231]\n" \
  "v_max3_u32 v60, v60, v144, v146\nv_mfma_f32_16x16x32_bf16 a[232:235], v[116:119], v[112:115], a[232:235]\n" \
  "v_max3_u32 v60, v60, v148, v150\nv_mfma_f32_16x16x32_bf16 a[236:239], v[120:123], v[112:115], a[236:239]\n" \
  "buffer_load_dwordx4 v[212:215], %[va], %[ra], %[k] offen offset:1024 sc1\ns_add_u32 %[k], %[k], 5120\n" \
  "v_mfma_f32_16x16x32_bf16 a[240:243], v[124:127], v[112:115], a[240:243]\nv_max3_u32 v60, v60, v152, v154\n" \
  "v_mfma_f32_16x16x32_bf16 a[244:247], v[128:131], v[112:115], a[244:247]\nv_cmp_eq_u32 vcc, -1, v60\n" \
  "v_mfma_f32_16x16x32_bf16 a[248:251], v[132:135], v[112:115], a[248:251]\ns_cbranch_vccnz 108b\n" \
  "208:\ns_waitcnt lgkmcnt(0)\n" \
  "v_mfma_f32_16x16x32_bf16 v[64:67], v[136:139], v[84:87], v[64:67]\nbuffer_load_dwordx4 v[216:219], %[va], %[ra], %[k] offen offset:0 sc1\n" \
  "v_mfma_f32_16x16x32_bf16 v[68:71], v[140:143], v[84:87], v[68:71]\nv_mfma_f32_16x16x32_bf16 v[72:75], v[144:147], v[84:87], v[72:75]\n" \
  "v_mfma_f32_16x16x32_bf16 v[76:79], v[148:151], v[84:87], v[76:79]\nv_mfma_f32_16x16x32_bf16 v[80:83], v[152:155], v[84:87], v[80:83]\n" \
  "buffer_load_dwordx4 v[220:223], %[va], %[ra], %[k] offen offset:256 sc1\nv_mfma_f32_16x16x32_bf16 a[192:195], v[136:139], v[88:91], a[192:195]\n" \
  "v_mfma_f32_16x16x32_bf16 a[196:199], v[140:143], v[88:91], a[196:199]\nv_mfma_f32_16x16x32_bf16 a[200:203], v[144:147], v[88:91], a[200:203]\n" \
  "v_mfma_f32_16x16x32_bf16 a[204:207], v[148:151], v[88:91], a[204:207]\nbuffer_load_dwordx4 v[224:227], %[va], %[ra], %[k] offen offset:512 sc1\n" \
  "v_mfma_f32_16x16x32_bf16 a[208:211], v[152:155], v[88:91], a[208:211]\nv_mfma_f32_16x16x32_bf16 a[212:215], v[136:139], v[92:95], a[212:215]\n" \
  "v_mfma_f32_16x16x32_bf16 a[216:219], v[140:143], v[92:95], a[216:219]\ns_waitcnt vmcnt(13)\n" \
  "v_max_u32 v60, v156, v158\nv_mfma_f32_16x16x32_bf16 a[220:223], v[144:147], v[92:95], a[220:223]\n" \
  "buffer_load_dwordx4 v[228:231], %[va], %[ra], %[k] offen offset:768 sc1\nv_mfma_f32_16x16x32_bf16 a[224:227], v[148:151], v[92:95], a[224:227]\n" \
  "v_max3_u32 v60, v60, v160, v162\nv_mfma_f32_16x16x32_bf16 a[228:231], v[152:155], v[92:95], a[228:231]\n" \
  "v_max3_u32 v60, v60, v164, v166\nv_mfma_f32_16x16x32_bf16 a[232:235], v[136:139], v[96:99], a[232:235]\n" \
  "v_max3_u32 v60, v60, v168, v170\nv_mfma_f32_16x16x32_bf16 a[236:239], v[140:143], v[96:99], a[236:239]\n" \
  "buffer_load_dwordx4 v[232:235], %[va], %[ra], %[k] offen offset:1024 sc1\ns_add_u32 %[k], %[k], 5120\n" \
  "v_mfma_f32_16x16x32_bf16 a[240:243], v[144:147], v[96:99], a[240:243]\nv_max3_u32 v60, v60, v172, v174\n" \
  "v_mfma_f32_16x16x32_bf16 a[244:247], v[148:151], v[96:99], a[244:247]\nv_cmp_eq_u32 vcc, -1, v60\n" \
  "v_mfma_f32_16x16x32_bf16 a[248:251], v[152:155], v[96:99], a[248:251]\ns_cbranch_vccnz 109b\n" \
  "209:\nv_mfma_f32_16x16x32_bf16 a[192:195], v[156:159], a[4:7], a[192:195]\n" \
  "buffer_load_dwordx4 v[236:239], %[va], %[ra], %[k] offen offset:0 sc1\nv_mfma_f32_16x16x32_bf16 a[196:199], v[160:163], a[4:7], a[196:199]\n" \
  "v_mfma_f32_16x16x32_bf16 a[200:203], v[164:167], a[4:7], a[200:203]\nv_mfma_f32_16x16x32_bf16 a[204:207], v[168:171], a[4:7], a[204:207]\n" \
  "buffer_load_dwordx4 v[240:243], %[va], %[ra], %[k] offen offset:256 sc1\nv_mfma_f32_16x16x32_bf16 a[208:211], v[172:175], a[4:7], a[208:211]\n" \
  "v_mfma_f32_16x16x32_bf16 a[212:215], v[156:159], a[8:11], a[212:215]\nv_mfma_f32_16x16x32_bf16 a[216:219], v[160:163], a[8:11], a[216:219]\n" \
  "buffer_load_dwordx4 v[244:247], %[va], %[ra], %[k] offen offset:512 sc1\nv_mfma_f32_16x16x32_bf16 a[220:223], v[164:167], a[8:11], a[220:223]\n" \
  "s_waitcnt vmcnt(13)\nv_max_u32 v60, v176, v178\n" \
  "v_mfma_f32_16x16x32_bf16 a[224:227], v[168:171], a[8:11], a[224:227]\nv_max3_u32 v60, v60, v180, v182\n" \
  "v_mfma_f32_16x16x32_bf16 a[228:231], v[172:175], a[8:11], a[228:231]\nbuffer_load_dwordx4 v[248:251], %[va], %[ra], %[k] offen offset:768 sc1\n" \
  "v_mfma_f32_16x16x32_bf16 a[232:235], v[156:159], a[12:15], a[232:235]\nv_max3_u32 v60, v60, v184, v186\n" \
  "v_mfma_f32_16x16x32_bf16 a[236:239], v[160:163], a[12:15], a[236:239]\nv_max3_u32 v60, v60, v188, v190\n" \
  "v_mfma_f32_16x16x32_bf16 a[240:243], v[164:167], a[12:15], a[240:243]\nbuffer_load_dwordx4 v[252:255], %[va], %[ra], %[k] offen offset:1024 sc1\n" \
  "s_add_u32 %[k], %[k], 5120\nv_mfma_f32_16x16x32_bf16 a[244:247], v[168:171], a[12:15], a[244:247]\n" \
  "v_max3_u32 v60, v60, v192, v194\nv_mfma_f32_16x16x32_bf16 a[248:251], v[172:175], a[12:15], a[248:251]\n" \
  "v_cmp_eq_u32 vcc, -1, v60\ns_cbranch_vccnz 110b\n" \
  "210:\ns_barrier\n" \
  "v_mfma_f32_16x16x32_bf16 a[192:195], v[176:179], a[20:23], a[192:195]\nbuffer_load_dwordx2 v[116:117], %[vx], %[rx], 0 offen offset:0\n" \
  "v_mfma_f32_16x16x32_bf16 a[196:199], v[180:183], a[20:23], a[196:199]\nv_mfma_f32_16x16x32_bf16 a[200:203], v[184:187], a[20:23], a[200:203]\n" \
  "v_mfma_f32_16x16x32_bf16 a[204:207], v[188:191], a[20:23], a[204:207]\nbuffer_load_dwordx2 v[120:121], %[vx], %[rx], 0 offen offset:32\n" \
  "v_mfma_f32_16x16x32_bf16 a[208:211], v[192:195], a[20:23], a[208:211]\nv_mfma_f32_16x16x32_bf16 a[212:215], v[176:179], a[24:27], a[212:215]\n" \
  "v_mfma_f32_16x16x32_bf16 a[216:219], v[180:183], a[24:27], a[216:219]\nbuffer_load_dwordx2 v[124:125], %[vx], %[rx], 0 offen offset:64\n" \
  "v_mfma_f32_16x16x32_bf16 a[220:223], v[184:187], a[24:27], a[220:223]\ns_waitcnt vmcnt(13)\n" \
  "v_max_u32 v60, v196, v198\nv_mfma_f32_16x16x32_bf16 a[224:227], v[188:191], a[24:27], a[224:227]\n" \
  "v_max3_u32 v60, v60, v200, v202\nv_mfma_f32_16x16x32_bf16 a[228:231], v[192:195], a[24:27], a[228:231]\n" \
  "buffer_load_dwordx2 v[128:129], %[vx], %[rx], 0 offen offset:96\nv_mfma_f32_16x16x32_bf16 a[232:235], v[176:179], a[28:31], a[232:235]\n" \
  "v_max3_u32 v60, v60, v204, v206\nv_mfma_f32_16x16x32_bf16 a[236:239], v[180:183], a[28:31], a[236:239]\n" \
  "v_max3_u32 v60, v60, v208, v210\nv_mfma_f32_16x16x32_bf16 a[240:243], v[184:187], a[28:31], a[240:243]\n" \
  "buffer_load_dwordx2 v[132:133], %[vx], %[rx], 0 offen offset:128\nv_mfma_f32_16x16x32_bf16 a[244:247], v[188:191], a[28:31], a[244:247]\n" \
  "v_max3_u32 v60, v60, v212, v214\nv_mfma_f32_16x16x32_bf16 a[248:251], v[192:195], a[28:31], a[248:251]\n" \
  "v_cmp_eq_u32 vcc, -1, v60\ns_cbranch_vccnz 111b\n" \
  "211:\nv_mfma_f32_16x16x32_bf16 a[192:195], v[196:199], a[36:39], a[192:195]\n" \
  "buffer_load_dwordx2 v[118:119], %[vy], %[ry], 0 offen offset:0 sc1\nv_mfma_f32_16x16x32_bf16 a[196:199], v[200:203], a[36:39], a[196:199]\n" \
  "v_mfma_f32_16x16x32_bf16 a[200:203], v[204:207], a[36:39], a[200:203]\nv_mfma_f32_16x16x32_bf16 a[204:207], v[208:211], a[36:39], a[204:207]\n" \
  "buffer_load_dwordx2 v[122:123], %[vy], %[ry], 0 offen offset:256 sc1\nv_mfma_f32_16x16x32_bf16 a[208:211], v[212:215], a[36:39], a[208:211]\n" \
  "v_mfma_f32_16x16x32_bf16 a[212:215], v[196:199], a[40:43], a[212:215]\nv_mfma_f32_16x16x32_bf16 a[216:219], v[200:203], a[40:43], a[216:219]\n" \
  "buffer_load_dwordx2 v[126:127], %[vy], %[ry], 0 offen offset:512 sc1\nv_mfma_f32_16x16x32_bf16 a[220:223], v[204:207], a[40:43], a[220:223]\n" \
  "s_waitcnt vmcnt(13)\nv_max_u32 v60, v216, v218\n" \
  "v_mfma_f32_16x16x32_bf16 a[224:227], v[208:211], a[40:43], a[224:227]\nv_max3_u32 v60, v60, v220, v222\n" \
  "v_mfma_f32_16x16x32_bf16 a[228:231], v[212:215], a[40:43], a[228:231]\nbuffer_load_dwordx2 v[130:131], %[vy], %[ry], 0 offen offset:768 sc1\n" \
  "v_mfma_f32_16x16x32_bf16 a[232:235], v[196:199], a[44:47], a[232:235]\nv_max3_u32 v60, v60, v224, v226\n" \
  "v_mfma_f32_16x16x32_bf16 a[236:239], v[200:203], a[44:47], a[236:239]\nv_max3_u32 v60, v60, v228, v230\n" \
  "v_mfma_f32_16x16x32_bf16 a[240:243], v[204:207], a[44:47], a[240:243]\nbuffer_load_dwordx2 v[134:135], %[vy], %[ry], 0 offen offset:1024 sc1\n" \
  "v_mfma_f32_16x16x32_bf16 a[244:247], v[208:211], a[44:47], a[244:247]\nv_max3_u32 v60, v60, v232, v234\n" \
  "v_mfma_f32_16x16x32_bf16 a[248:251], v[212:215], a[44:47], a[248:251]\nv_cmp_eq_u32 vcc, -1, v60\n" \
  "s_cbranch_vccnz 112b\n212:\n" \
  "v_mfma_f32_16x16x32_bf16 a[192:195], v[216:219], a[52:55], a[192:195]\nv_mfma_f32_16x16x32_bf16 a[196:199], v[220:223], a[52:55], a[196:199]\n" \
  "v_mfma_f32_16x16x32_bf16 a[200:203], v[224:227], a[52:55], a[200:203]\nv_mfma_f32_16x16x32_bf16 a[204:207], v[228:231], a[52:55], a[204:207]\n" \
  "v_mfma_f32_16x16x32_bf16 a[208:211], v[232:235], a[52:55], a[208:211]\nv_mfma_f32_16x16x32_bf16 a[212:215], v[216:219], a[56:59], a[212:215]\n" \
  "v_mfma_f32_16x16x32_bf16 a[216:219], v[220:223], a[56:59], a[216:219]\ns_waitcnt vmcnt(10)\n" \
  "v_max_u32 v60, v236, v238\nv_mfma_f32_16x16x32_bf16 a[220:223], v[224:227], a[56:59], a[220:223]\n" \
  "v_max3_u32 v60, v60, v240, v242\nv_mfma_f32_16x16x32_bf16 a[224:227], v[228:231], a[56:59], a[224:227]\n" \
  "v_max3_u32 v60, v60, v244, v246\nv_mfma_f32_16x16x32_bf16 a[228:231], v[232:235], a[56:59], a[228:231]\n" \
  "v_max3_u32 v60, v60, v248, v250\nv_mfma_f32_16x16x32_bf16 a[232:235], v[216:219], a[60:63], a[232:235]\n" \
  "v_max3_u32 v60, v60, v252, v254\nv_mfma_f32_16x16x32_bf16 a[236:239], v[220:223], a[60:63], a[236:239]\n" \
  "v_cmp_eq_u32 vcc, -1, v60\nv_mfma_f32_16x16x32_bf16 a[240:243], v[224:227], a[60:63], a[240:243]\n" \
  "v_mfma_f32_16x16x32_bf16 a[244:247], v[228:231], a[60:63], a[244:247]\nv_mfma_f32_16x16x32_bf16 a[248:251], v[232:235], a[60:63], a[248:251]\n" \
  "s_cbranch_vccnz 113b\n213:\n" \
  "v_mfma_f32_16x16x32_bf16 a[192:195], v[236:239], a[68:71], a[192:195]\nv_mfma_f32_16x16x32_bf16 a[196:199], v[240:243], a[68:71], a[196:199]\n" \
  "v_mfma_f32_16x16x32_bf16 a[200:203], v[244:247], a[68:71], a[200:203]\nv_mfma_f32_16x16x32_bf16 a[204:207], v[248:251], a[68:71], a[204:207]\n" \
  "v_mfma_f32_16x16x32_bf16 a[208:211], v[252:255], a[68:71], a[208:211]\nv_mfma_f32_16x16x32_bf16 a[212:215], v[236:239], a[72:75], a[212:215]\n" \
  "v_mfma_f32_16x16x32_bf16 a[216:219], v[240:243], a[72:75], a[216:219]\nv_mfma_f32_16x16x32_bf16 a[220:223], v[244:247], a[72:75], a[220:223]\n" \
  "v_mfma_f32_16x16x32_bf16 a[224:227], v[248:251], a[72:75], a[224:227]\nds_write_b128 v61, a[192:195] offset:0\n" \
  "v_mfma_f32_16x16x32_bf16 a[228:231], v[252:255], a[72:75], a[228:231]\nv_mfma_f32_16x16x32_bf16 a[232:235], v[236:239], a[76:79], a[232:235]\n" \
  "ds_write_b128 v61, a[196:199] offset:1024\nv_mfma_f32_16x16x32_bf16 a[236:239], v[240:243], a[76:79], a[236:239]\n" \
  "v_mfma_f32_16x16x32_bf16 a[240:243], v[244:247], a[76:79], a[240:243]\nds_write_b128 v61, a[200:203] offset:2048\n" \
  "v_mfma_f32_16x16x32_bf16 a[244:247], v[248:251], a[76:79], a[244:247]\nv_mfma_f32_16x16x32_bf16 a[248:251], v[252:255], a[76:79], a[248:251]\n" \
  "ds_write_b128 v61, a[204:207] offset:3072\nv_mfma_f32_16x16x32_bf16 v[64:67], v[156:159], a[0:3], v[64:67]\n" \
  "v_mfma_f32_16x16x32_bf16 v[68:71], v[160:163], a[0:3], v[68:71]\nds_write_b128 v61, a[208:211] offset:4096\n" \
  "v_mfma_f32_16x16x32_bf16 v[72:75], v[164:167], a[0:3], v[72:75]\nv_mfma_f32_16x16x32_bf16 v[76:79], v[168:171], a[0:3], v[76:79]\n" \
  "ds_write_b128 v62, a[212:215] offset:5120\nv_mfma_f32_16x16x32_bf16 v[80:83], v[172:175], a[0:3], v[80:83]\n" \
  "v_mfma_f32_16x16x32_bf16 v[64:67], v[176:179], a[16:19], v[64:67]\nds_write_b128 v62, a[216:219] offset:6144\n" \
  "v_mfma_f32_16x16x32_bf16 v[68:71], v[180:183], a[16:19], v[68:71]\nv_mfma_f32_16x16x32_bf16 v[72:75], v[184:187], a[16:19], v[72:75]\n" \
  "ds_write_b128 v62, a[220:223] offset:7168\nv_mfma_f32_16x16x32_bf16 v[76:79], v[188:191], a[16:19], v[76:79]\n" \
  "v_mfma_f32_16x16x32_bf16 v[80:83], v[192:195], a[16:19], v[80:83]\nds_write_b128 v62, a[224:227] offset:8192\n" \
  "v_mfma_f32_16x16x32_bf16 v[64:67], v[196:199], a[32:35], v[64:67]\nv_mfma_f32_16x16x32_bf16 v[68:71], v[200:203], a[32:35], v[68:71]\n" \
  "ds_write_b128 v62, a[228:231] offset:9216\nv_mfma_f32_16x16x32_bf16 v[72:75], v[204:207], a[32:35], v[72:75]\n" \
  "v_mfma_f32_16x16x32_bf16 v[76:79], v[208:211], a[32:35], v[76:79]\nds_write_b128 v63, a[232:235] offset:10240\n" \
  "v_mfma_f32_16x16x32_bf16 v[80:83], v[212:215], a[32:35], v[80:83]\nv_mfma_f32_16x16x32_bf16 v[64:67], v[216:219], a[48:51], v[64:67]\n" \
  "ds_write_b128 v63, a[236:239] offset:11264\nv_mfma_f32_16x16x32_bf16 v[68:71], v[220:223], a[48:51], v[68:71]\n" \
  "v_mfma_f32_16x16x32_bf16 v[72:75], v[224:227], a[48:51], v[72:75]\nds_write_b128 v63, a[240:243] offset:12288\n" \
  "v_mfma_f32_16x16x32_bf16 v[76:79], v[228:231], a[48:51], v[76:79]\nv_mfma_f32_16x16x32_bf16 v[80:83], v[232:235], a[48:51], v[80:83]\n" \
  "ds_write_b128 v63, a[244:247] offset:13312\nv_mfma_f32_16x16x32_bf16 v[64:67], v[236:239], a[64:67], v[64:67]\n" \
  "v_mfma_f32_16x16x32_bf16 v[68:71], v[240:243], a[64:67], v[68:71]\nds_write_b128 v63, a[248:251] offset:14336\n" \
  "s_mul_i32 %[u], %[wv], 40960\nv_add_u32 v60, %[u], %[vl]\n" \
  "v_mfma_f32_16x16x32_bf16 v[72:75], v[244:247], a[64:67], v[72:75]\nv_mfma_f32_16x16x32_bf16 v[76:79], v[248:251], a[64:67], v[76:79]\n" \
  "v_mfma_f32_16x16x32_bf16 v[80:83], v[252:255], a[64:67], v[80:83]\ns_waitcnt lgkmcnt(0)\n" \
  "s_barrier\nds_read_b128 v[84:87], v60 offset:0\n" \
  "ds_read_b128 v[88:91], v60 offset:5120\nds_read_b128 v[92:95], v60 offset:10240\n" \
  "ds_read_b128 v[96:99], v60 offset:1024\nds_read_b128 v[100:103], v60 offset:6144\n" \
  "ds_read_b128 v[104:107], v60 offset:11264\nds_read_b128 v[108:111], v60 offset:2048\n" \
  "ds_read_b128 v[112:115], v60 offset:7168\nds_read_b128 v[136:139], v60 offset:12288\n" \
  "ds_read_b128 v[140:143], v60 offset:3072\nds_read_b128 v[144:147], v60 offset:8192\n" \
  "ds_read_b128 v[148:151], v60 offset:13312\nds_read_b128 v[152:155], v60 offset:4096\n" \
  "ds_read_b128 v[156:159], v60 offset:9216\nds_read_b128 v[160:163], v60 offset:14336\n" \
  "s_waitcnt vmcnt(0)\ns_waitcnt lgkmcnt(12)\n" \
  "v_pk_add_f32 v[84:85], v[84:85], v[88:89]\nv_pk_add_f32 v[86:87], v[86:87], v[90:91]\n" \
  "v_pk_add_f32 v[64:65], v[64:65], v[92:93]\nv_pk_add_f32 v[66:67], v[66:67], v[94:95]\n" \
  "v_pk_add_f32 v[64:65], v[64:65], v[84:85]\nv_pk_add_f32 v[66:67], v[66:67], v[86:87]\n" \
  "s_waitcnt lgkmcnt(9)\nv_pk_add_f32 v[96:97], v[96:97], v[100:101]\n" \
  "v_pk_add_f32 v[98:99], v[98:99], v[102:103]\nv_pk_add_f32 v[68:69], v[68:69], v[104:105]\n" \
  "v_pk_add_f32 v[70:71], v[70:71], v[106:107]\nv_pk_add_f32 v[68:69], v[68:69], v[96:97]\n" \
  "v_pk_add_f32 v[70:71], v[70:71], v[98:99]\ns_waitcnt lgkmcnt(6)\n" \
  "v_pk_add_f32 v[108:109], v[108:109], v[112:113]\nv_pk_add_f32 v[110:111], v[110:111], v[114:115]\n" \
  "v_pk_add_f32 v[72:73], v[72:73], v[136:137]\nv_pk_add_f32 v[74:75], v[74:75], v[138:139]\n" \
  "v_pk_add_f32 v[72:73], v[72:73], v[108:109]\nv_pk_add_f32 v[74:75], v[74:75], v[110:111]\n" \
  "s_waitcnt lgkmcnt(3)\nv_pk_add_f32 v[140:141], v[140:141], v[144:145]\n" \
  "v_pk_add_f32 v[142:143], v[142:143], v[146:147]\nv_pk_add_f32 v[76:77], v[76:77], v[148:149]\n" \
  "v_pk_add_f32 v[78:79], v[78:79], v[150:151]\nv_pk_add_f32 v[76:77], v[76:77], v[140:141]\n" \
  "v_pk_add_f32 v[78:79], v[78:79], v[142:143]\ns_waitcnt lgkmcnt(0)\n" \
  "v_pk_add_f32 v[152:153], v[152:153], v[156:157]\nv_pk_add_f32 v[154:155], v[154:155], v[158:159]\n" \
  "v_pk_add_f32 v[80:81], v[80:81], v[160:161]\nv_pk_add_f32 v[82:83], v[82:83], v[162:163]\n" \
  "v_pk_add_f32 v[80:81], v[80:81], v[152:153]\nv_pk_add_f32 v[82:83], v[82:83], v[154:155]\n" \
  ""
#define CONV_CLOBBERS \
  "v56", "v57", "v58", "v59", "v60", "v61", "v62", "v63", "v96", "v97", "v98", "v99", "v100", "v101", "v102", "v103", "v104", "v105", "v106", "v107", \
  "v108", "v109", "v110", "v111", "v112", "v113", "v114", "v115", "v116", "v117", "v118", "v119", "v120", "v121", "v122", "v123", "v124", "v125", "v126", "v127", \
  "v128", "v129", "v130", "v131", "v132", "v133", "v134", "v135", "v136", "v137", "v138", "v139", "v140", "v141", "v142", "v143", "v144", "v145", "v146", "v147", \
  "v148", "v149", "v150", "v151", "v152", "v153", "v154", "v155", "v156", "v157", "v158", "v159", "v160", "v161", "v162", "v163", "v164", "v165", "v166", "v167", \
  "v168", "v169", "v170", "v171", "v172", "v173", "v174", "v175", "v176", "v177", "v178", "v179", "v180", "v181", "v182", "v183", "v184", "v185", "v186", "v187", \
  "v188", "v189", "v190", "v191", "v192", "v193", "v194", "v195", "v196", "v197", "v198", "v199", "v200", "v201", "v202", "v203", "v204", "v205", "v206", "v207", \
  "v208", "v209", "v210", "v211", "v212", "v213", "v214", "v215", "v216", "v217", "v218", "v219", "v220", "v221", "v222", "v223", "a0", "a1", "a2", "a3", \
  "a4", "a5", "a6", "a7", "a8", "a9", "a10", "a11", "a12", "a13", "a14", "a15", "a16", "a17", "a18", "a19", "a20", "a21", "a22", "a23", \
  "a24", "a25", "a26", "a27", "a28", "a29", "a30", "a31", "a32", "a33", "a34", "a35", "a36", "a37", "a38", "a39", "a40", "a41", "a42", "a43", \
  "a44", "a45", "a46", "a47", "a48", "a49", "a50", "a51", "a52", "a53", "a54", "a55", "a56", "a57", "a58", "a59", "a60", "a61", "a62", "a63", \
  "a64", "a65", "a66", "a67", "a68", "a69", "a70", "a71", "a72", "a73", "a74", "a75", "a76", "a77", "a78", "a79", "a80", "a81", "a82", "a83", \
  "a84", "a85", "a86", "a87", "a88", "a89", "a90", "a91", "a92", "a93", "a94", "a95", "a96", "a97", "a98", "a99", "a100", "a101", "a102", "a103", \
  "a104", "a105", "a106", "a107", "a108", "a109", "a110", "a111", "a112", "a113", "a114", "a115", "a116", "a117", "a118", "a119", "a120", "a121", "a122", "a123", \
  "a124", "a125", "a126", "a127", "a128", "a129", "a130", "a131", "a132", "a133", "a134", "a135", "a136", "a137", "a138", "a139", "a140", "a141", "a142", "a143", \
  "a144", "a145", "a146", "a147", "a148", "a149", "a150", "a151", "a152", "a153", "a154", "a155", "a156", "a157", "a158", "a159", "a160", "a161", "a162", "a163", \
  "a164", "a165", "a166", "a167", "a168", "a169", "a170", "a171", "a172", "a173", "a174", "a175", "a176", "a177", "a178", "a179", "a180", "a181", "a182", "a183", \
  "a184", "a185", "a186", "a187", "a188", "a189", "a190", "a191", "a192", "a193", "a194", "a195", "a196", "a197", "a198", "a199", "a200", "a201", "a202", "a203", \
  "a204", "a205", "a206", "a207", "a208", "a209", "a210", "a211", "a212", "a213", "a214", "a215", "a216", "a217", "a218", "a219", "a220", "a221", "a222", "a223", \
  "a224", "a225", "a226", "a227", "a228", "a229", "a230", "a231", "a232", "a233", "a234", "a235", "a236", "a237", "a238", "a239", "a240", "a241", "a242", "a243", \
  "a244", "a245", "a246", "a247", "a248", "a249", "a250", "a251", "a252", "a253", "a254", "a255", \
  "vcc", "scc", "memory"
#define M1_CLOBBERS \
  "v56", "v57", "v58", "v59", "v60", "a0", "a1", "a2", "a3", "a4", "a5", "a6", "a7", "a8", "a9", "a10", "a11", "a12", "a13", "a14", \
  "a15", "a16", "a17", "a18", "a19", "a20", "a21", "a22", "a23", "a24", "a25", "a26", "a27", "a28", "a29", "a30", "a31", "a32", "a33", "a34", \
  "a35", "a36", "a37", "a38", "a39", "a40", "a41", "a42", "a43", "a44", "a45", "a46", "a47", "a48", "a49", "a50", "a51", "a52", "a53", "a54", \
  "a55", "a56", "a57", "a58", "a59", "a60", "a61", "a62", "a63", "a64", "a65", "a66", "a67", "a68", "a69", "a70", "a71", "a72", "a73", "a74", \
  "a75", "a76", "a77", "a78", "a79", "a80", "a81", "a82", "a83", "a84", "a85", "a86", "a87", "a88", "a89", "a90", "a91", "a92", "a93", "a94", \
  "a95", "a96", "a97", "a98", "a99", "a100", "a101", "a102", "a103", "a104", "a105", "a106", "a107", "a108", "a109", "a110", "a111", "a112", "a113", "a114", \
  "a115", "a116", "a117", "a118", "a119", "a120", "a121", "a122", "a123", "a124", "a125", "a126", "a127", "a128", "a129", "a130", "a131", "a132", "a133", "a134", \
  "a135", "a136", "a137", "a138", "a139", "a140", "a141", "a142", "a143", "a144", "a145", "a146", "a147", "a148", "a149", "a150", "a151", "a152", "a153", "a154", \
  "a155", "a156", "a157", "a158", "a159", "a160", "a161", "a162", "a163", "a164", "a165", "a166", "a167", "a168", "a169", "a170", "a171", "a172", "a173", "a174", \
  "a175", "a176", "a177", "a178", "a179", "a180", "a181", "a182", "a183", "a184", "a185", "a186", "a187", "a188", "a189", "a190", "a191", "a192", "a193", "a194", \
  "a195", "a196", "a197", "a198", "a199", "a200", "a201", "a202", "a203", "a204", "a205", "a206", "a207", "a208", "a209", "a210", "a211", "a212", "a213", "a214", \
  "a215", "a216", "a217", "a218", "a219", "a220", "a221", "a222", "a223", "a224", "a225", "a226", "a227", "a228", "a229", "a230", "a231", "a232", "a233", "a234", \
  "a235", "a236", "a237", "a238", "a239", "a240", "a241", "a242", "a243", "a244", "a245", "a246", "a247", "a248", "a249", "a250", "a251", "a252", "a253", "a254", \
  "a255", \
  "vcc", "scc", "memory"
#define G1_CLOBBERS \
  "v56", "v57", "v58", "v59", "v60", "v61", "v62", "v63", "v84", "v85", "v86", "v87", "v88", "v89", "v90", "v91", "v92", "v93", "v94", "v95", \
  "v96", "v97", "v98", "v99", "v100", "v101", "v102", "v103", "v104", "v105", "v106", "v107", "v108", "v109", "v110", "v111", "v112", "v113", "v114", "v115", \
  "v116", "v117", "v118", "v119", "v120", "v121", "v122", "v123", "v124", "v125", "v126", "v127", "v128", "v129", "v130", "v131", "v132", "v133", "v134", "v135", \
  "v136", "v137", "v138", "v139", "v140", "v141", "v142", "v143", "v144", "v145", "v146", "v147", "v148", "v149", "v150", "v151", "v152", "v153", "v154", "v155", \
  "v156", "v157", "v158", "v159", "v160", "v161", "v162", "v163", "v164", "v165", "v166", "v167", "v168", "v169", "v170", "v171", "v172", "v173", "v174", "v175", \
  "v176", "v177", "v178", "v179", "v180", "v181", "v182", "v183", "v184", "v185", "v186", "v187", "v188", "v189", "v190", "v191", "v192", "v193", "v194", "v195", \
  "v196", "v197", "v198", "v199", "v200", "v201", "v202", "v203", "v204", "v205", "v206", "v207", "v208", "v209", "v210", "v211", "v212", "v213", "v214", "v215", \
  "v216", "v217", "v218", "v219", "v220", "v221", "v222", "v223", "v224", "v225", "v226", "v227", "v228", "v229", "v230", "v231", "v232", "v233", "v234", "v235", \
  "v236", "v237", "v238", "v239", "v240", "v241", "v242", "v243", "v244", "v245", "v246", "v247", "v248", "v249", "v250", "v251", "v252", "v253", "v254", "v255", \
  "a0", "a1", "a2", "a3", "a4", "a5", "a6", "a7", "a8", "a9", "a10", "a11", "a12", "a13", "a14", "a15", "a16", "a17", "a18", "a19", \
  "a20", "a21", "a22", "a23", "a24", "a25", "a26", "a27", "a28", "a29", "a30", "a31", "a32", "a33", "a34", "a35", "a36", "a37", "a38", "a39", \
  "a40", "a41", "a42", "a43", "a44", "a45", "a46", "a47", "a48", "a49", "a50", "a51", "a52", "a53", "a54", "a55", "a56", "a57", "a58", "a59", \
  "a60", "a61", "a62", "a63", "a64", "a65", "a66", "a67", "a68", "a69", "a70", "a71", "a72", "a73", "a74", "a75", "a76", "a77", "a78", "a79", \
  "a80", "a81", "a82", "a83", "a84", "a85", "a86", "a87", "a88", "a89", "a90", "a91", "a92", "a93", "a94", "a95", "a96", "a97", "a98", "a99", \
  "a100", "a101", "a102", "a103", "a104", "a105", "a106", "a107", "a108", "a109", "a110", "a111", "a112", "a113", "a114", "a115", "a116", "a117", "a118", "a119", \
  "a120", "a121", "a122", "a123", "a124", "a125", "a126", "a127", "a128", "a129", "a130", "a131", "a132", "a133", "a134", "a135", "a136", "a137", "a138", "a139", \
  "a140", "a141", "a142", "a143", "a144", "a145", "a146", "a147", "a148", "a149", "a150", "a151", "a152", "a153", "a154", "a155", "a156", "a157", "a158", "a159", \
  "a160", "a161", "a162", "a163", "a164", "a165", "a166", "a167", "a168", "a169", "a170", "a171", "a172", "a173", "a174", "a175", "a176", "a177", "a178", "a179", \
  "a180", "a181", "a182", "a183", "a184", "a185", "a186", "a187", "a188", "a189", "a190", "a191", "a192", "a193", "a194", "a195", "a196", "a197", "a198", "a199", \
  "a200", "a201", "a202", "a203", "a204", "a205", "a206", "a207", "a208", "a209", "a210", "a211", "a212", "a213", "a214", "a215", "a216", "a217", "a218", "a219", \
  "a220", "a221", "a222", "a223", "a224", "a225", "a226", "a227", "a228", "a229", "a230", "a231", "a232", "a233", "a234", "a235", "a236", "a237", "a238", "a239", \
  "a240", "a241", "a242", "a243", "a244", "a245", "a246", "a247", "a248", "a249", "a250", "a251", "a252", "a253", "a254", "a255", \
  "vcc", "scc", "memory"
#define G2_CLOBBERS \
  "v56", "v57", "v58", "v59", "v60", "v61", "v62", "v63", "v84", "v85", "v86", "v87", "v88", "v89", "v90", "v91", "v92", "v93", "v94", "v95", \
  "v96", "v97", "v98", "v99", "v100", "v101", "v102", "v103", "v104", "v105", "v106", "v107", "v108", "v109", "v110", "v111", "v112", "v113", "v114", "v115", \
  "v136", "v137", "v138", "v139", "v140", "v141", "v142", "v143", "v144", "v145", "v146", "v147", "v148", "v149", "v150", "v151", "v152", "v153", "v154", "v155", \
  "v156", "v157", "v158", "v159", "v160", "v161", "v162", "v163", "v164", "v165", "v166", "v167", "v168", "v169", "v170", "v171", "v172", "v173", "v174", "v175", \
  "v176", "v177", "v178", "v179", "v180", "v181", "v182", "v183", "v184", "v185", "v186", "v187", "v188", "v189", "v190", "v191", "v192", "v193", "v194", "v195", \
  "v196", "v197", "v198", "v199", "v200", "v201", "v202", "v203", "v204", "v205", "v206", "v207", "v208", "v209", "v210", "v211", "v212", "v213", "v214", "v215", \
  "v216", "v217", "v218", "v219", "v220", "v221", "v222", "v223", "v224", "v225", "v226", "v227", "v228", "v229", "v230", "v231", "v232", "v233", "v234", "v235", \
  "v236", "v237", "v238", "v239", "v240", "v241", "v242", "v243", "v244", "v245", "v246", "v247", "v248", "v249", "v250", "v251", "v252", "v253", "v254", "v255", \
  "a0", "a1", "a2", "a3", "a4", "a5", "a6", "a7", "a8", "a9", "a10", "a11", "a12", "a13", "a14", "a15", "a16", "a17", "a18", "a19", \
  "a20", "a21", "a22", "a23", "a24", "a25", "a26", "a27", "a28", "a29", "a30", "a31", "a32", "a33", "a34", "a35", "a36", "a37", "a38", "a39", \
  "a40", "a41", "a42", "a43", "a44", "a45", "a46", "a47", "a48", "a49", "a50", "a51", "a52", "a53", "a54", "a55", "a56", "a57", "a58", "a59", \
  "a60", "a61", "a62", "a63", "a64", "a65", "a66", "a67", "a68", "a69", "a70", "a71", "a72", "a73", "a74", "a75", "a76", "a77", "a78", "a79", \
  "a80", "a81", "a82", "a83", "a84", "a85", "a86", "a87", "a88", "a89", "a90", "a91", "a92", "a93", "a94", "a95", "a96", "a97", "a98", "a99", \
  "a100", "a101", "a102", "a103", "a104", "a105", "a106", "a107", "a108", "a109", "a110", "a111", "a112", "a113", "a114", "a115", "a116", "a117", "a118", "a119", \
  "a120", "a121", "a122", "a123", "a124", "a125", "a126", "a127", "a128", "a129", "a130", "a131", "a132", "a133", "a134", "a135", "a136", "a137", "a138", "a139", \
  "a140", "a141", "a142", "a143", "a144", "a145", "a146", "a147", "a148", "a149", "a150", "a151", "a152", "a153", "a154", "a155", "a156", "a157", "a158", "a159", \
  "a160", "a161", "a162", "a163", "a164", "a165", "a166", "a167", "a168", "a169", "a170", "a171", "a172", "a173", "a174", "a175", "a176", "a177", "a178", "a179", \
  "a180", "a181", "a182", "a183", "a184", "a185", "a186", "a187", "a188", "a189", "a190", "a191", "a192", "a193", "a194", "a195", "a196", "a197", "a198", "a199", \
  "a200", "a201", "a202", "a203", "a204", "a205", "a206", "a207", "a208", "a209", "a210", "a211", "a212", "a213", "a214", "a215", "a216", "a217", "a218", "a219", \
  "a220", "a221", "a222", "a223", "a224", "a225", "a226", "a227", "a228", "a229", "a230", "a231", "a232", "a233", "a234", "a235", "a236", "a237", "a238", "a239", \
  "a240", "a241", "a242", "a243", "a244", "a245", "a246", "a247", "a248", "a249", "a250", "a251", "a252", "a253", "a254", "a255", \
  "vcc", "scc", "memory"

__device__ __forceinline__ float bflo(u32 v) { return __builtin_bit_cast(float, v << 16); }
__device__ __forceinline__ float bfhi(u32 v) { return __builtin_bit_cast(float, v & 0xffff0000u); }
__device__ __forceinline__ float bf2f(u16 v) { return __builtin_bit_cast(float, (u32)v << 16); }
__device__ __forceinline__ f32x2 unpk(u32 v) { return (f32x2){bflo(v), bfhi(v)}; }
__device__ __forceinline__ u32 pk_bf16(float lo, float hi) {
  f32x2 a = {lo, hi};
  return __builtin_bit_cast(u32, __builtin_convertvector(a, bf16x2));
}
__device__ __forceinline__ float rbf(float x) { return bflo(pk_bf16(x, x)); }
__device__ __forceinline__ u32 pk_f16(float lo, float hi) {
  f32x2 a = {lo, hi};
  return __builtin_bit_cast(u32, __builtin_convertvector(a, f16x2));
}
template <int CTRL> __device__ __forceinline__ float dpp(float x) {
  return __builtin_bit_cast(float, __builtin_amdgcn_mov_dpp(__builtin_bit_cast(int, x), CTRL, 0xf, 0xf, true));
}
constexpr int XOR1 = 0xB1, XOR2 = 0x4E, ROR8 = 0x128, ROR4 = 0x124, WSHL = 0x130, WSHR = 0x138;
__device__ __forceinline__ float next_lane(float x, float last) {
  return __builtin_bit_cast(float, __builtin_amdgcn_update_dpp(__builtin_amdgcn_readfirstlane(__builtin_bit_cast(int, last)),
                                                               __builtin_bit_cast(int, x), WSHL, 0xf, 0xf, false));
}
__device__ __forceinline__ float half_sum(float x) {
  x += dpp<XOR1>(x);
  x += dpp<XOR2>(x);
  x += dpp<ROR4>(x);
  x += dpp<ROR8>(x);
  const auto s = __builtin_amdgcn_permlane16_swap(__builtin_bit_cast(u32, x), __builtin_bit_cast(u32, x), false, false);
  return __builtin_bit_cast(float, (u32)s[0]) + __builtin_bit_cast(float, (u32)s[1]);
}
__device__ __forceinline__ float wave_max(float x) {
  x = fmaxf(x, dpp<XOR1>(x)); x = fmaxf(x, dpp<XOR2>(x)); x = fmaxf(x, dpp<ROR4>(x)); x = fmaxf(x, dpp<ROR8>(x));
  auto s = __builtin_amdgcn_permlane16_swap(__builtin_bit_cast(u32, x), __builtin_bit_cast(u32, x), false, false);
  x = fmaxf(__builtin_bit_cast(float, (u32)s[0]), __builtin_bit_cast(float, (u32)s[1]));
  auto t = __builtin_amdgcn_permlane32_swap(__builtin_bit_cast(u32, x), __builtin_bit_cast(u32, x), false, false);
  return fmaxf(__builtin_bit_cast(float, (u32)t[0]), __builtin_bit_cast(float, (u32)t[1]));
}
__device__ __forceinline__ u32x2 tr_read(const char* lds_ptr) {
  return __builtin_bit_cast(u32x2, __builtin_amdgcn_ds_read_tr16_b64_v4bf16((LAS bf16x4*)lds_ptr));
}
__device__ __forceinline__ rsrc_t mk(const void* p, int bytes) { return __builtin_amdgcn_make_buffer_rsrc((void*)p, 0, bytes, 0x00020000); }
__device__ __forceinline__ u32 nofill(u32 v) { return min(v, 0xfffffffeu); }
__device__ __forceinline__ u32x4 nofill4(u32x2 a, u32x2 b) { return (u32x4){nofill(a.x), a.y, nofill(b.x), b.y}; }

template <int N> using IC = std::integral_constant<int, N>;
constexpr int NG = 5;
__device__ __forceinline__ u32 pk2(f32x2 a) { return pk_bf16(a.x, a.y); }
template <class F0, class F1> __device__ __forceinline__ float ln_var(f32x2 (&v)[10], F0 f0, F1 f1) {
  f32x2 s = (v[0] + v[1]) + (v[2] + v[3]);
  f32x2 s2 = (v[4] + v[5]) + (v[6] + v[7]);
  s = (s + s2) + (v[8] + v[9]);
  const float mean = half_sum(s.x + s.y) * (1.0f / D);
  f0();
  f32x2 q[4];
#pragma unroll
  for (int i = 0; i < 10; ++i) {
    v[i] -= mean;
    q[i & 3] = i < 4 ? v[i] * v[i] : q[i & 3] + v[i] * v[i];
  }
  q[0] = (q[0] + q[1]) + (q[2] + q[3]);
  f1();
  return half_sum(q[0].x + q[0].y) * (1.0f / D);
}
__device__ __forceinline__ float ln_var(f32x2 (&v)[10]) { return ln_var(v, []() {}, []() {}); }
__device__ __forceinline__ float ln_den(float var) { return rbf(__builtin_amdgcn_sqrtf(rbf(rbf(var) + EPS))); }

struct cplx { f32x2 re, im; };
__device__ __forceinline__ cplx cmul(cplx a, cplx b) {
  cplx r;
  r.re = a.re * b.re - a.im * b.im;
  r.im = a.re * b.im + a.im * b.re;
  return r;
}
__device__ __forceinline__ cplx cexp_p(f32x2 a2, f32x2 brev, float p) {
  const f32x2 e = a2 * __builtin_fabsf(p), g = brev * p;
  const f32x2 mag = {__builtin_amdgcn_exp2f(e.x), __builtin_amdgcn_exp2f(e.y)};
  const float a0 = __builtin_amdgcn_fractf(g.x), a1 = __builtin_amdgcn_fractf(g.y);
  cplx r;
  r.re = mag * (f32x2){__builtin_amdgcn_cosf(a0), __builtin_amdgcn_cosf(a1)};
  r.im = mag * (f32x2){__builtin_amdgcn_sinf(a0), __builtin_amdgcn_sinf(a1)};
  return r;
}
__device__ __forceinline__ void put_row(char* p, int row, cplx v) {
  *(u32x2*)(p + row * KG_ROW) = (u32x2){pk_f16(v.re.x, v.im.x), pk_f16(v.re.y, v.im.y)};
}
__device__ __forceinline__ float gelu_tanh(float x) {
  const float z = x * (-2.3022081986f - 0.1029432397f * x * x);
  return x * __builtin_amdgcn_rcpf(1.0f + __builtin_amdgcn_exp2f(z));
}
__device__ __forceinline__ u32x4 pair8(u32x2 a, u32x2 b) {
  const auto x = __builtin_amdgcn_permlane16_swap(a.x, b.x, false, false);
  const auto y = __builtin_amdgcn_permlane16_swap(a.y, b.y, false, false);
  return (u32x4){(u32)x[0], (u32)y[0], (u32)x[1], (u32)y[1]};
}

__global__ __launch_bounds__(256, 1) void fused(const u16* __restrict__ x, const u16* __restrict__ logA, const u16* __restrict__ Aim,
                                                const u16* __restrict__ Cre, const u16* __restrict__ Cim, const u16* __restrict__ logdt,
                                                const u16* __restrict__ dskip, const u16* __restrict__ w1, const u16* __restrict__ b1,
                                                const u16* __restrict__ w2, const u16* __restrict__ b2, char* ws, u16* out) {
  extern __shared__ __attribute__((aligned(16))) char smem[];
  int tid = threadIdx.x, lane = tid & 63;
  const int blk = blockIdx.x, xcd = blk & 7, slot = blk >> 3;
  const int ncv = (int)((CNT >> (8 * xcd)) & 255);
  const bool conv = slot < ncv;
  const int cg = conv ? (int)((BASE >> (8 * xcd)) & 255) + slot : 160;
  const rsrc_t rsX = mk(x, SZ_ACT), rsY = mk(ws + OFF_Y, 256 * YB);
  u32x16 q0;
  u32x2 pdt4, pds4;
  u32 tag;
  asm volatile(P0_ASM
               : "=&{v[96:111]}"(q0), [ep] "=&{s90}"(tag), "=&{s[92:93]}"(pdt4), "=&{s[94:95]}"(pds4)
               : [bk] "s"(cg), [ws] "s"(ws), [pdt] "s"(logdt), [pds] "s"(dskip), [d2] "s"(cg * 8), [v0] "v"(cg * 1024 + tid * 4), [a] "s"(logA),
                 [b] "s"(Aim), [c] "s"(Cre), [d] "s"(Cim), [vx] "v"(((tid >> 6) + ((tid >> 3) & 4)) * (D * 2) + (tid & 31) * 8), [rx] "s"(rsX),
                 [s0] "s"(blk * 10240), "s"(w1), "s"(b1), "s"(w2), "s"(b2), "s"(out)
               : "scc", "memory");
  PIN();
  {
    const u32x4 ones = {~0u, ~0u, ~0u, ~0u};
    __builtin_amdgcn_raw_buffer_store_b128(ones, rsY, tid * 16, blk * YB, 16);
    __builtin_amdgcn_raw_buffer_store_b128(ones, rsY, tid * 16, blk * YB + 4096, 16);
    __builtin_amdgcn_raw_buffer_store_b128(ones, rsY, (tid & 127) * 16, blk * YB + 8192, 16);
  }
  PIN();
  u32 cpf;
  asm volatile("s_getpc_b64 s[96:97]\n1:\n\ts_mov_b32 s98, fused_end-1b\n\ts_mov_b32 s99, 0x20000\n\ts_and_b32 s97, s97, 0xffff\n\t"
               "buffer_load_dword %0, %1, s[96:99], %2 offen"
               : "=&v"(cpf)
               : "v"(min(tid, CPL - 1) * 128), "s"((slot & (CPG - 1)) * (CPL * 128))
               : "s96", "s97", "s98", "s99", "scc");
  PIN();
  const int wave = __builtin_amdgcn_readfirstlane(tid >> 6), d = cg * 4 + wave;
  const int mt0 = (xcd & 1) * 4 + (slot & 3), nt1 = (xcd >> 1) * 7 + (slot >> 2), nt2 = (xcd >> 1) * 8 + (slot >> 2);
  const bool act = (slot >> 2) < 7 && nt1 < 27;
  int i16 = lane & 15, kg = lane >> 4, l32 = lane & 31, hh = lane >> 5;
  const rsrc_t rsU = mk(ws + OFF_U, 2 * SZ_ACT);
  const int lds0 = (int)(size_t)(LAS char*)smem, ldsw = lds0 + wave * WREG;
  char* const wb = smem + wave * WREG;
  int t, k, n, u, se;
  auto mix = [](u32 e) { return e * 0x9e3779b1u + 0x7f4a7c15u; };
  const rsrc_t rsW1 = mk(w1, act ? L * F * 2 : 0), rsW2 = mk(w2, L * F * 2);
  const int wsw = ((lane & 7) ^ ((lane >> 3) & 2)) << 4;
  const int vw1 = (lane >> 3) * (F * 2) + wsw, vw2 = (lane >> 3) * (L * 2) + wsw;
  const int sw1 = wave * 512 * (F * 2) + nt1 * 128, sw2 = wave * 448 * (L * 2) + nt2 * 128;
  const int rb = ldsw + kg * WPIECE + (i16 >> 2) * 128 + (lane & 3) * 8, xc = (wave ^ ((lane >> 3) & 1)) << 5;
  const rsrc_t rsP = mk(w1, L * F * 2);
  const int vp1 = ((slot & 3) * 512 + (tid >> 3)) * (F * 2) + (xcd >> 1) * 896 + min(tid & 7, 6) * 128;
  const int vp2 = ((slot & 3) * 448 + (tid >> 3)) * (L * 2) + (xcd >> 1) * 1024 + (tid & 7) * 128;
  u32 pf0;
  const int sp1 = ((slot >> 2) + 4) * (64 * F);
  asm volatile(P1_ASM
               : "+{v[96:111]}"(q0), "+{s90}"(tag), "+{s[92:93]}"(pdt4), "+{s[94:95]}"(pds4), [pf] "=&{v225}"(pf0)
               : [v1] "v"(vp1), [rp] "s"(rsP), [s] "s"(sp1)
               : "memory");
  tag = mix(tag);
  u32x2 bb = __builtin_amdgcn_raw_buffer_load_b64(mk(b1, F * 2), kg * 8 + nt1 * 128 + wave * 32, 0, 0);
  u32 bias2 = __builtin_amdgcn_raw_buffer_load_b16(mk(b2, L * 2), (nt2 * 64 + 16 * wave + i16) * 2, 0, 0);
  PIN();
  u32x4 q1;
  const int vx = (wave + 4 * hh) * (D * 2) + l32 * 8, vy = l32 * 64 + wave * 16 + hh * 8;
  auto ln_u = [&]() {
    f32x2 v[10];
#pragma unroll
    for (int j = 0; j < 10; ++j) v[j] = unpk(q0[j]);
    const float rd = __builtin_amdgcn_rcpf(ln_den(ln_var(v)));
#pragma unroll
    for (int j = 0; j < 5; ++j) *(u32x2*)(wb + XTRA + wave * 64 + hh * TROW + l32 * 8 + j * 256) = (u32x2){pk2(v[2 * j] * rd), pk2(v[2 * j + 1] * rd)};
  };
  u32x2 g[5];
  auto get_u = [&]() {
    asm volatile("s_waitcnt vmcnt(%1) lgkmcnt(0)\n\ts_barrier" ::"v"(cpf), "n"(PF0 + 2) : "memory");
#pragma unroll
    for (int r = 0; r < 5; ++r) g[r] = tr_read(smem + XTRA + (i16 >> 2) * (WREG + 64) + (lane & 3) * 8 + (kg & 1) * TROW + (tid >> 5) * 32 + r * 256);
    PIN();
  };
  auto put_u = [&](int r) {
    PIN();
    __builtin_amdgcn_raw_buffer_store_b128((u32x4){g[r].x, g[r].y, tag, tag}, rsU, ((tid >> 5) * 4 + (i16 >> 2)) * 32768 + (kg & 1) * 64 + (lane & 3) * 16,
                                           blk * 128 + r * (32 * 32768), 16);
    PIN();
  };
  if (conv) {
    u32x3 ug[8];
    auto load_u = [&]() {
#pragma unroll
      for (int i = 0; i < 8; ++i) ug[i] = __builtin_amdgcn_raw_buffer_load_b96(rsU, lane * 16 + (i & 3) * 1024, cg * 32768 + wave * 8192 + (i >> 2) * 4096, 16);
    };
    const u32 pA = q0[10], pB = q0[11], pCr = q0[12], pCi = q0[13], pdt2 = (wave & 2) ? pdt4.y : pdt4.x, pds2 = (wave & 2) ? pds4.y : pds4.x;
    ln_u();
    const float dt = rbf(expf((wave & 1) ? bfhi(pdt2) : bflo(pdt2)));
    const f32x2 la = unpk(pA);
    const f32x2 a2 = ((f32x2){-rbf(expf(la.x)), -rbf(expf(la.y))} * dt) * 1.44269504088896340736f;
    const f32x2 brev = (unpk(pB) * dt) * 0.15915494309189533577f;
    cplx c = {unpk(pCr) * dt, unpk(pCi) * dt};
    const float mx = wave_max(fmaxf(fmaxf(fabsf(c.re.x), fabsf(c.re.y)), fmaxf(fabsf(c.im.x), fabsf(c.im.y))));
    const u32 eb = __builtin_bit_cast(u32, fmaxf(mx, 1e-30f)) & 0x7f800000u;
    const float us = 2.0f * __builtin_bit_cast(float, eb), scale = __builtin_bit_cast(float, 0x7f000000u - eb);
    c.re *= scale; c.im *= scale;
    get_u();
    const cplx w = cexp_p(a2, brev, -8.f);
    put_u(0);
    const cplx w4 = cexp_p(a2, brev, -32.f);
    put_u(1);
    const cplx z = cexp_p(a2, brev, 1.f);
    put_u(2);
    const cplx vv = cexp_p(a2, brev, 256.f);
    put_u(3);
    char* tb = wb + lane * 8;
    {
      cplx p[4];
      p[0] = {{1.f, 1.f}, {0.f, 0.f}}; p[1] = w; p[2] = cmul(w, w); p[3] = cmul(p[2], w);
#pragma unroll
      for (int g = 0; g < 8; ++g) {
        if (g == 0) { put_u(4); }
        if (PF0 == 0 && g == RG) {
          PIN();
          asm volatile("buffer_load_dword %0, %1, %2, %3 offen" : "=&{v225}"(pf0) : "v"(vp1), "s"(rsP), "s"(sp1) : "memory");
          PIN();
        }
#pragma unroll
        for (int j = 0; j < 4; ++j) {
          put_row(tb, 21 + 4 * g + j, p[j]);
          if (g < 7) p[j] = cmul(p[j], w4);
        }
      }
    }
    auto wfence = []() {
      __builtin_amdgcn_fence(__ATOMIC_RELEASE, "wavefront");
      __builtin_amdgcn_wave_barrier();
      __builtin_amdgcn_fence(__ATOMIC_ACQUIRE, "wavefront");
    };
    wfence();
    f16x8 fl[16], fh[16];
#pragma unroll
    for (int ks = 0; ks < 16; ++ks) fl[ks] = *(const f16x8*)(wb + (21 + l32) * KG_ROW + hh * 16 + ks * 32);
    PIN();
    {
      cplx ch[8];
      const cplx z2 = cmul(z, z);
      ch[0] = c; ch[1] = cmul(c, z);
#pragma unroll
      for (int h = 2; h < 8; ++h) ch[h] = cmul(ch[h - 2], z2);
#pragma unroll
      for (int h = 0; h < 8; ++h) put_row(tb, h, ch[h]);
      cplx e0 = cmul(ch[0], vv), e4 = cmul(ch[4], vv);
      put_row(tb, 13, e0); put_row(tb, 14, cmul(ch[2], vv)); put_row(tb, 15, e4); put_row(tb, 16, cmul(ch[6], vv));
      e0 = cmul(e0, vv); e4 = cmul(e4, vv);
      put_row(tb, 8, e0); put_row(tb, 9, e4);
      e0 = cmul(e0, vv); e4 = cmul(e4, vv);
      put_row(tb, 17, e0); put_row(tb, 18, e4);
      e0 = cmul(e0, vv); put_row(tb, 10, e0);
      e0 = cmul(e0, vv); put_row(tb, 19, e0);
      e0 = cmul(e0, vv); put_row(tb, 11, e0);
      e0 = cmul(e0, vv); put_row(tb, 20, e0);
      e0 = cmul(e0, vv); put_row(tb, 12, e0);
    }
    PIN();
    wfence();
    f32x16 kc = {}, kd = {};
    {
      const int ri = 4 * (l32 >> 3) + (l32 & 3);
      const int row = (l32 & 4) ? 13 + (int)((0x765433222100ull >> (4 * ri)) & 15) : min(ri, 12);
#pragma unroll
      for (int ks = 0; ks < 16; ++ks) fh[ks] = *(const f16x8*)(wb + row * KG_ROW + hh * 16 + ks * 32);
      PIN();
#pragma unroll
      for (int ks = 0; ks < 16; ks += 2) {
        kc = __builtin_amdgcn_mfma_f32_32x32x16_f16(fh[ks], fl[ks], kc, 0, 0, 0);
        kd = __builtin_amdgcn_mfma_f32_32x32x16_f16(fh[ks + 1], fl[ks + 1], kd, 0, 0, 0);
      }
    }
    PIN();
    load_u();
    PIN();
    wfence();
    {
      float v[13];
#pragma unroll
      for (int i = 0; i < 13; ++i) v[i] = (kc[i] + kd[i]) * us;
      const float n0 = next_lane(v[0], v[8]), p2 = next_lane(v[8], v[10]), b0 = next_lane(v[10], v[11]), b1n = next_lane(v[11], v[12]);
      if (hh) v[7] = n0;
      if (lane == 0) v[0] += (wave & 1) ? bfhi(pds2) : bflo(pds2);
      const float km = dpp<WSHR>(v[7]);
      char* r0 = wb + R_OFF + (63 - lane) * 16;
      char* r1 = r0 + CV_R;
      *(u32x4*)(r1 + 3072) = (u32x4){pk_bf16(v[7], v[6]), pk_bf16(v[5], v[4]), pk_bf16(v[3], v[2]), pk_bf16(v[1], v[0])};
      *(u32x4*)(r0 + 3072) = (u32x4){pk_bf16(v[6], v[5]), pk_bf16(v[4], v[3]), pk_bf16(v[2], v[1]), pk_bf16(v[0], km)};
      const u32 q00 = pk_bf16(v[8], v[8]), q11 = pk_bf16(v[9], v[9]);
      *(u32x4*)(r1 + 2048) = (u32x4){pk_bf16(p2, p2), q11, pk_bf16(v[9], v[8]), q00};
      *(u32x4*)(r0 + 2048) = (u32x4){pk_bf16(p2, v[9]), q11, q00, q00};
      const float t0 = (lane & 1) ? b0 : v[10], t1 = (lane & 1) ? b1n : v[11];
      const u32 aa0 = pk_bf16(v[10], v[10]), bb0 = pk_bf16(b0, b0), aa1 = pk_bf16(v[11], v[11]), bb1 = pk_bf16(b1n, b1n);
      *(u32x4*)(r1 + 1024) = (u32x4){bb0, pk_bf16(b0, t0), aa0, aa0};
      *(u32x4*)(r0 + 1024) = (u32x4){bb0, pk_bf16(t0, v[10]), aa0, aa0};
      *(u32x4*)(r1) = (u32x4){bb1, pk_bf16(b1n, t1), aa1, aa1};
      *(u32x4*)(r0) = (u32x4){bb1, pk_bf16(t1, v[11]), aa1, aa1};
    }
    PIN();
#pragma nounroll
    for (int it = 0; it < SPIN; ++it) {
      u32 bad = 0;
#pragma unroll
      for (int i = 0; i < 8; ++i) bad |= ug[i].z ^ tag;
      if (__builtin_amdgcn_ballot_w64(bad != 0) == 0) break;
      asm volatile("" ::: "memory");
#pragma unroll
      for (int i = 0; i < 8; ++i)
        if (ug[i].z != tag) ug[i] = __builtin_amdgcn_raw_buffer_load_b96(rsU, lane * 16 + (i & 3) * 1024, cg * 32768 + wave * 8192 + (i >> 2) * 4096, 16);
    }
    __builtin_amdgcn_s_waitcnt(0x0f70);
    asm volatile("s_barrier" : "+v"(bb), "+v"(bias2) : "{v225}"(pf0) : "memory");
    {
      char* up = smem + (lane & 3) * (WREG + 32) + U_OFF + 448 + wave * 1024 + (lane >> 2) * 8;
#pragma unroll
      for (int i = 0; i < 8; ++i) {
        *(u32x2*)(up + i * 128 + CV_U) = (u32x2){ug[i].x, ug[i].y};
        *(u32*)(up + i * 128 + 4) = ug[i].x;
        *(u32*)(up + i * 128 + 8) = ug[i].y;
      }
      const u32x4 zz = {0, 0, 0, 0};
      const int zo = l32 < 28 ? U_OFF + wave * 32 + hh * CV_U + l32 * 16 : R_OFF + hh * CV_R + 4096 + (l32 - 28) * 16;
      *(u32x4*)(wb + zo) = zz;
      if (lane == 0) *(u32*)(wb + U_OFF + wave * 32 + 448) = 0;
    }
    asm volatile("s_waitcnt lgkmcnt(0)\n\ts_barrier" ::: "memory");
    f32x4 ya[8];
    {
      const int tp = ldsw + R_OFF + (i16 & 1) * CV_R + 2 * (28 - 4 * (i16 >> 1) + 8 * kg);
      const int up = ldsw + U_OFF + wave * 32 + (i16 & 1) * CV_U + 2 * (32 * (i16 >> 1) + 8 * kg);
      asm volatile(CONV_ASM
                   : "=&{v[64:67]}"(ya[0]), "=&{v[68:71]}"(ya[1]), "=&{v[72:75]}"(ya[2]), "=&{v[76:79]}"(ya[3]), "=&{v[80:83]}"(ya[4]),
                     "=&{v[84:87]}"(ya[5]), "=&{v[88:91]}"(ya[6]), "=&{v[92:95]}"(ya[7]), [t] "=&s"(t), [k] "=&s"(k)
                   : [tp0] "v"(tp), [up] "v"(up), [vw] "v"(vw1), [rw] "s"(rsW1), [sw] "s"(sw1), [l] "s"(ldsw),
                     [rb] "v"(rb), [xc] "v"(xc),
                     [vh] "v"(lane * 16), [rh] "s"(mk(ws + OFF_HN, (L + F) * D * 2)), [sh] "s"(d * 8192)
                   : CONV_CLOBBERS);
    }
    {
      char* yb = wb + XTRA + wave * 8 + (4 * (i16 >> 1) + kg) * 16 + (i16 & 1) * 4;
#pragma unroll
      for (int A = 0; A < 8; ++A) {
        *(u32*)(yb + A * 512) = pk_bf16(ya[A][0], ya[A][1]);
        *(u32*)(yb + A * 512 + 8) = pk_bf16(ya[A][2], ya[A][3]);
      }
      asm volatile("s_waitcnt lgkmcnt(0)\n\ts_barrier" ::: "memory");
      const char* rp = smem + XTRA + (i16 >> 2) * (WREG + 8) + (tid >> 4) * 64 + (lane & 3) * 16;
#pragma unroll
      for (int i = 0; i < 4; ++i) {
        const u32x2 a = tr_read(rp + i * 1024), b = tr_read(rp + i * 1024 + 8);
        __builtin_amdgcn_raw_buffer_store_b128(nofill4(a, b), rsY, (tid >> 2) * YB + (tid & 3) * 16, cg * 64 + i * (64 * YB), 16);
      }
    }
    asm volatile(M1_ASM
                 : [t] "=&s"(t), [k] "=&s"(k), "=&{v[96:111]}"(q0), "=&{v[112:115]}"(q1)
                 : [vx] "v"(vx), [rx] "s"(rsX), [sx] "s"(blk * 10240), [vy] "v"(vy), [ry] "s"(rsY), [sy] "s"(blk * YB), [vw] "v"(vw1), [rb] "v"(rb), [xc] "v"(xc), [rw] "s"(rsW1), [sw] "s"(sw1), [l] "s"(ldsw)
                 : M1_CLOBBERS);
  } else {
    ln_u();
    get_u();
#pragma unroll
    for (int r = 0; r < 5; ++r) put_u(r);
    const int oi = min((slot - ncv) * 256 + tid, 2559);
    const u32 ot = __builtin_amdgcn_raw_buffer_load_b32(mk(out, SZ_ACT), oi / 5 * (D * 2) + oi % 5 * 128, (xcd >> 1) * (512 * D * 2) + (xcd & 1) * 640, 0);
    PIN();
    asm volatile(HELP_ASM
                 : [t] "=&s"(t), [k] "=&s"(k), "=&{v[96:111]}"(q0), "=&{v[112:115]}"(q1)
                 : [vw] "v"(vw1), [rb] "v"(rb), [xc] "v"(xc), [rw] "s"(rsW1), [sw] "s"(sw1), [l] "s"(ldsw),
                   "{v225}"(pf0), [v1] "v"(vp1), [rp] "s"(rsP), [s1] "s"(sp1), [ja] "s"(((19 - (slot >> 2)) & 15) * (64 * F)), [v2] "v"(vp2), [rw2] "s"(rsW2),
                   [hq] "s"(7 - (slot >> 2)), [nh] "s"(8 - (ncv >> 2)),
                   [vq] "v"((lane & 7) * 8 + ((blk * 37 + wave * 40) % 160) * 64), [vx] "v"(vx), [rx] "s"(rsX), [sx] "s"(blk * 10240), [vy] "v"(vy),
                   [ry] "s"(rsY), [sy] "s"(blk * YB)
                 : M1_CLOBBERS);
    asm volatile("" : "+v"(bb), "+v"(bias2) : "v"(ot));
  }
  asm volatile("" : "+v"(tid));
  lane = tid & 63; i16 = lane & 15; kg = lane >> 4; l32 = lane & 31; hh = lane >> 5;
  const int s = nt2 * 64 + 16 * wave + i16;
  {
    u32x2 xr[5], yr[5];
#pragma unroll
    for (int j = 0; j < 5; ++j) {
      xr[j] = (u32x2){q0[2 * j], q0[2 * j + 1]};
      yr[j] = j < 3 ? (u32x2){q0[10 + 2 * j], q0[11 + 2 * j]} : (u32x2){q1[2 * j - 6], q1[2 * j - 5]};
    }
#pragma nounroll
    for (int it = 0; it < SPIN; ++it) {
      const u32 m = max(max(max(yr[0].x, yr[1].x), max(yr[2].x, yr[3].x)), yr[4].x);
      if (__builtin_amdgcn_ballot_w64(m == ~0u) == 0) break;
      asm volatile("" ::: "memory");
#pragma unroll
      for (int j = 0; j < 5; ++j)
        if (yr[j].x == ~0u) yr[j] = __builtin_amdgcn_raw_buffer_load_b64(rsY, l32 * 64 + wave * 16 + hh * 8 + (j & 1) * 2048, blk * YB + (j >> 1) * 4096, 16);
      __builtin_amdgcn_s_waitcnt(0x0f70);
    }
    if (conv) asm volatile(M2_ASM : [k] "=&s"(k) : [rb] "v"(rb), [xc] "v"(xc) : M1_CLOBBERS);
    auto dma = [&](auto G) {
      constexpr int per = (NL2 + NG - 1) / NG, p0 = NW - 12 + (G.value < NG ? G.value * per : NL2), p1 = G.value < NG ? (p0 + per < NW - 12 + NL2 ? p0 + per : NW - 12 + NL2) : 28;
      PIN();
      if (conv || G.value == NG) {
#pragma unroll
        for (int p = p0; p < p1; ++p) {
          if (p == 20) asm volatile("s_waitcnt lgkmcnt(0)" ::: "memory");
          asm volatile("s_add_u32 m0, %1, %2\n\ts_add_u32 %0, %3, %4\n\tbuffer_load_dwordx4 %5, %6, %0 offen lds"
                       : "=&s"(t)
                       : "s"(ldsw), "n"(p * WPIECE), "s"(p < 12 ? sw1 : sw2), "n"(p < 12 ? (4 + p) * 16 * F : (24 + p) * 16 * L), "v"(p < 12 ? vw1 : vw2),
                         "s"(p < 12 ? rsW1 : rsW2)
                       : "scc", "memory");
        }
      }
      PIN();
    };
    dma(IC<0>{});
    f32x2 v[10];
#pragma unroll
    for (int j = 0; j < 5; ++j) {
      v[2 * j] = unpk(pk2(unpk(xr[j].x) + unpk(yr[j].x)));
      v[2 * j + 1] = unpk(pk2(unpk(xr[j].y) + unpk(yr[j].y)));
    }
    dma(IC<1>{});
    const float var = ln_var(v, [&]() { dma(IC<2>{}); }, [&]() { dma(IC<3>{}); });
    dma(IC<4>{});
    const float r1 = __builtin_amdgcn_rcpf(ln_den(var));
    const float rd = r1 * __builtin_amdgcn_rcpf(ln_den(var * r1 * r1));
    char* const twp = wb + XTRA + wave * 64 + hh * TROW + l32 * 8;
#pragma unroll
    for (int j = 0; j < 5; ++j) *(u32x2*)(twp + j * 256) = (u32x2){pk2(v[2 * j] * rd), pk2(v[2 * j + 1] * rd)};
    asm volatile("s_waitcnt lgkmcnt(0)\n\ts_barrier" ::: "memory");
    const rsrc_t rsH = mk(ws + OFF_HN, SZ_ACT);
    const char* rp = smem + XTRA + (i16 >> 2) * (WREG + 64) + (lane & 3) * 8 + (tid >> 4) * 32;
#pragma unroll
    for (int r = 0; r < 3; ++r) {
      if (r < 2 || wave < 2) {
        const int dd = tid + 256 * r, q = (dd * 205) >> 14;
        const u32x2 a = tr_read(rp + r * 512), b = tr_read(rp + r * 512 + TROW);
        __builtin_amdgcn_raw_buffer_store_b128(nofill4(a, b), rsH, q * (256 * 1280 - 1280) + dd * 16, blk * 1280, 16);
      }
    }
    dma(IC<NG>{});
  }
  const int va = kg * 1280 + i16 * 16, vl = lds0 + 4 * WSLOT + lane * 16;
  f32x16 ow0;
  f32x4 ow1;
  u32 pf1 = 0;
  auto own = [&](int mt, int j) { return mt < 4 ? ow0[4 * mt + j] : ow1[j]; };
  if (!act) {
    asm volatile(IDLE_ASM
                 : [t] "=&s"(t), [k] "=&s"(k)
                 : [rb] "v"(rb), [xc] "v"(xc), [vw2] "v"(vw2), [rw2] "s"(rsW2), [sw2] "s"(sw2), [l] "s"(ldsw), [v2] "v"(vp2)
                 : M1_CLOBBERS);
  } else {
    const rsrc_t rsA = mk(ws + OFF_HN + mt0 * (256 * 1280), 256 * 1280);
    asm volatile(G1_ASM
                 : "=&{v[64:79]}"(ow0), "=&{v[80:83]}"(ow1), [t] "=&s"(t), [k] "=&s"(k), [n] "=&s"(n), [u] "=&s"(u), [se] "=&s"(se), "=&{v55}"(pf1)
                 : [va] "v"(va), [vp] "v"(lane * 1280), [rb] "v"(rb), [xc] "v"(xc), [vl] "v"(vl), [vw2] "v"(vw2),
                   [vg] "v"(vp2 + ((slot >> 2) < 14 - JE ? ((slot >> 2) + JE - 5) << 17 : 1 << 30)), [ra] "s"(rsA), [rw2] "s"(rsW2), [sa] "s"(wave * 16 * 5120), [r0] "s"(((slot >> 2) * ROT % 16) * 5120), [sw2] "s"(sw2), [l] "s"(ldsw), [wv] "s"(wave)
                 : G1_CLOBBERS);
    u32x2 o[5];
    const f32x2 c0 = unpk(bb.x), c1 = unpk(bb.y);
#pragma unroll
    for (int mt = 0; mt < 5; ++mt) {
      o[mt].x = nofill(pk_bf16(gelu_tanh(own(mt, 0) + c0.x), gelu_tanh(own(mt, 1) + c0.y)));
      o[mt].y = pk_bf16(gelu_tanh(own(mt, 2) + c1.x), gelu_tanh(own(mt, 3) + c1.y));
    }
    const rsrc_t rsG = mk(ws + OFF_G + mt0 * (216 * 1280), 216 * 1280);
    const int vg = (kg >> 1) * 1280 + i16 * 16, so = (nt1 * 8 + 2 * wave) * 1280;
    __builtin_amdgcn_raw_buffer_store_b128(pair8(o[0], o[1]), rsG, vg + (kg & 1) * 256, so, 16);
    __builtin_amdgcn_raw_buffer_store_b128(pair8(o[2], o[3]), rsG, vg + (kg & 1) * 256 + 512, so, 16);
    __builtin_amdgcn_raw_buffer_store_b64(o[4], rsG, vg + (kg & 1) * 8 + 1024, so, 16);
  }
  u32x16 re0;
  u32x4 re1;
  {
    const rsrc_t rsA = mk(ws + OFF_G + mt0 * (216 * 1280), 216 * 1280);
    asm volatile(G2_ASM
                 : "=&{v[64:79]}"(ow0), "=&{v[80:83]}"(ow1), "=&{v[116:131]}"(re0), "=&{v[132:135]}"(re1), [t] "=&s"(t), [k] "=&s"(k), [n] "=&s"(n), [u] "=&s"(u), [se] "=&s"(se)
                 : [va] "v"(va), [vp] "v"(min(lane, 55) * 1280), [vw2] "v"(vw2), [rb] "v"(rb), [xc] "v"(xc), [vl] "v"(vl), [rw2] "s"(rsW2),
                   [ra] "s"(rsA), [sw2] "s"(sw2), [sa] "s"(wave * 14 * 5120), [r0] "s"(((slot >> 2) * ROT % 14) * 5120), [l] "s"(ldsw), [wv] "s"(wave),
                   [vx] "v"(s * (D * 2) + kg * 8 + mt0 * 160), [rx] "s"(rsX), "{v55}"(pf1),
                   [vy] "v"((s >> 3) * YB + kg * 64 + (s & 3) * 16 + ((s >> 2) & 1) * 8 + mt0 * 1280), [ry] "s"(rsY)
                 : G2_CLOBBERS);
  }
  u32x2 q[5];
  const float bias = bflo(bias2);
#pragma unroll
  for (int mt = 0; mt < 5; ++mt) {
    const u32x4 r = mt < 4 ? (u32x4){re0[4 * mt], re0[4 * mt + 1], re0[4 * mt + 2], re0[4 * mt + 3]} : re1;
    const f32x2 a = unpk(r.x) + unpk(r.z), b = unpk(r.y) + unpk(r.w);
    const f32x2 ha = unpk(pk_bf16(a.x, a.y)), hb = unpk(pk_bf16(b.x, b.y));
    q[mt].x = pk_bf16(ha.x + (own(mt, 0) + bias), ha.y + (own(mt, 1) + bias));
    q[mt].y = pk_bf16(hb.x + (own(mt, 2) + bias), hb.y + (own(mt, 3) + bias));
  }
  const int eo = s * D + mt0 * 80, e8 = eo + (kg & 1) * 16 + (kg >> 1) * 8;
  const rsrc_t rsO = mk(out, SZ_ACT);
  __builtin_amdgcn_raw_buffer_store_b128(pair8(q[0], q[1]), rsO, e8 * 2, 0, OAUX);
  __builtin_amdgcn_raw_buffer_store_b128(pair8(q[2], q[3]), rsO, e8 * 2 + 64, 0, OAUX);
  __builtin_amdgcn_raw_buffer_store_b64(q[4], rsO, eo * 2 + 128 + 8 * kg, 0, OAUX);
  if (blk == 0 && tid == 0) *(u32*)ws = tag;
  asm volatile("fused_end:");
}

extern "C" void kernel_launch(void* const* d_in, const int* in_sizes, int n_in, void* d_out, int out_size, void* d_ws, size_t ws_size,
                              hipStream_t stream) {
  (void)hipFuncSetAttribute((const void*)fused, hipFuncAttributeMaxDynamicSharedMemorySize, 4 * WREG);
  fused<<<256, 256, 4 * WREG, stream>>>((const u16*)d_in[0], (const u16*)d_in[1], (const u16*)d_in[2], (const u16*)d_in[3], (const u16*)d_in[4],
                                        (const u16*)d_in[5], (const u16*)d_in[6], (const u16*)d_in[8], (const u16*)d_in[9], (const u16*)d_in[10],
                                        (const u16*)d_in[11], (char*)d_ws, (u16*)d_out);
}
```
